```python
import torch

def _softmax(x, axis=-1):
    return torch.softmax(x, dim=axis)

def _gelu(x):
    return torch.nn.functional.gelu(x, approximate='tanh')

def _silu(x):
    return torch.nn.functional.silu(x)

def _sigmoid(x):
    return torch.sigmoid(x)

def _layernorm(x, axis=-1, eps=1e-05):
    m = x.mean(dim=axis, keepdim=True)
    v = x.var(dim=axis, keepdim=True, unbiased=False)
    return (x - m) / torch.sqrt(v + eps)

def _scatter_add(tgt, idx, src, axis=0):
    return tgt.clone().index_add_(axis, idx, src)

def _segment_sum(src, seg, n):
    out = torch.zeros((n,) + src.shape[1:], dtype=src.dtype)
    return out.index_add_(0, seg, src)

def _pad(x, pw):
    flat = tuple((v for pair in reversed(pw) for v in pair))
    return torch.nn.functional.pad(x, flat)
kernel_name = 'adaLN_cond__grouped_kv_mqa__dense_activation_relu__prompt'

def setup_inputs():

    _w = lambda a: (lambda t: t.to(torch.bfloat16) if t.is_floating_point() else t)(torch.as_tensor(a))

    def _A_setup():
        B, S, D, H = (1, 2048, 576, 32)
        Dh = D // H
        return {'x': _w((lambda *s: torch.randn(*s, dtype=torch.bfloat16) * 0.05)(B, S, D)), 'wq': _w((lambda *s: torch.randn(*s, dtype=torch.bfloat16) * 0.05)(D, H * Dh)), 'wk': _w((lambda *s: torch.randn(*s, dtype=torch.bfloat16) * 0.05)(D, Dh)), 'wv': _w((lambda *s: torch.randn(*s, dtype=torch.bfloat16) * 0.05)(D, Dh)), 'wo': _w((lambda *s: torch.randn(*s, dtype=torch.bfloat16) * 0.05)(H * Dh, D))}
    _A_in = _A_setup()

    def _B_setup():
        B, S, D, Dff = (1, 2048, 576, 4608)
        return {'x': _w((lambda *s: torch.randn(*s, dtype=torch.bfloat16) * 0.05)(B, S, D)), 'w1': _w((lambda *s: torch.randn(*s, dtype=torch.bfloat16) * 0.05)(D, Dff)), 'w2': _w((lambda *s: torch.randn(*s, dtype=torch.bfloat16) * 0.05)(Dff, D))}
    _B_in = _B_setup()
    return {'A_x': _A_in['x'], 'A_wq': _A_in['wq'], 'A_wk': _A_in['wk'], 'A_wv': _A_in['wv'], 'A_wo': _A_in['wo'], 'B_x': _B_in['x'], 'B_w1': _B_in['w1'], 'B_w2': _B_in['w2'], 'c': _w((lambda *s: torch.randn(*s, dtype=torch.bfloat16) * 0.1)(1, 576) * 8.0), 'w_mod': _w((lambda *s: torch.randn(*s, dtype=torch.bfloat16) * 0.1)(576, 6 * 576) * 576 ** (-0.5) * 8.0)}

def reference(A_x, A_wq, A_wk, A_wv, A_wo, B_x, B_w1, B_w2, c, w_mod):

    def _A_ref(x, wq, wk, wv, wo):
        B, S, D = x.shape
        H = 32
        Dh = D // H
        q = (lambda x, *d: x.permute(*d))((x @ wq).reshape(B, S, H, Dh), 0, 2, 1, 3)
        k = x @ wk
        v = x @ wv
        scores = torch.einsum('bhqd,bkd->bhqk', q, k) * Dh ** (-0.5)
        p = _softmax(scores, axis=-1)
        o = torch.einsum('bhqk,bkd->bhqd', p, v)
        return (lambda x, *d: x.permute(*d))(o, 0, 2, 1, 3).reshape(B, S, H * Dh) @ wo

    def _B_ref(x, w1, w2):
        h = torch.einsum('bsd,df->bsf', x, w1)
        zero = torch.zeros_like(h)
        h = torch.maximum(h, zero)
        return torch.einsum('bsf,fd->bsd', h, w2)
    x = A_x
    mod = (_silu(c) @ w_mod).reshape(c.shape[0], 6, 1, -1)
    sa, ca, ga, sb, cb, gb = (mod[:, 0].clone(), mod[:, 1].clone(), mod[:, 2].clone(), mod[:, 3].clone(), mod[:, 4].clone(), mod[:, 5].clone())
    h = _layernorm(x, axis=-1) * (1 + ca) + sa
    x = x + ga * _A_ref(h, A_wq, A_wk, A_wv, A_wo)
    h = _layernorm(x, axis=-1) * (1 + cb) + sb
    x = x + gb * _B_ref(h, B_w1, B_w2)
    return x
```

```cpp
#include <hip/hip_runtime.h>
#include <hip/hip_bf16.h>
#include <stdint.h>

typedef unsigned short u16;
typedef short bf16x8 __attribute__((ext_vector_type(8)));
typedef short s16x4 __attribute__((ext_vector_type(4)));
typedef float f32x2 __attribute__((ext_vector_type(2)));
typedef float f32x4 __attribute__((ext_vector_type(4)));
typedef float f32x16 __attribute__((ext_vector_type(16)));
typedef unsigned u32x2 __attribute__((ext_vector_type(2)));
typedef unsigned u32x4 __attribute__((ext_vector_type(4)));
typedef __bf16 bf2_t __attribute__((ext_vector_type(2)));
#define LAS __attribute__((address_space(3)))
#define DI __device__ __forceinline__
#define SBAR() __builtin_amdgcn_sched_barrier(0)
#define PIN(x) asm volatile("" : "+v"(x))

constexpr int S_ = 2048, D_ = 576, NH = 32, DH = 18, FF = 4608;
constexpr float QSCALE = 0.23570226039551584f * 1.4426950408889634f;
constexpr float LN_EPS = 1e-5f;

constexpr size_t WS_MOD = 0;
constexpr size_t WS_KMAX = 8192;
constexpr size_t WS_XH = 16384;
constexpr size_t WS_WQKV = WS_XH + 2359296;
constexpr size_t WS_WO = WS_WQKV + 705024;
constexpr size_t WS_WF = WS_WO + 663552;
constexpr size_t WS_QF = WS_WF + 10616832;
constexpr size_t WS_QF2 = WS_QF + 2097152;
constexpr size_t WS_KF = WS_QF2 + 524288;
constexpr size_t WS_KF2 = WS_KF + 65536;
constexpr size_t WS_VF = WS_KF2 + 8192;
constexpr size_t WS_X1 = WS_VF + 73728;
constexpr size_t WS_H2 = WS_X1 + 2359296;
constexpr size_t WS_PART = WS_H2 + 2359296;
constexpr size_t WS_END = WS_PART + 18874368;

DI float bf2f(unsigned b) { return __uint_as_float(b << 16); }
DI float bflo(unsigned v) { return __uint_as_float(v << 16); }
DI float bfhi(unsigned v) { return __uint_as_float(v & 0xffff0000u); }
DI unsigned pk2(float a, float b) { f32x2 v = {a, b}; return __builtin_bit_cast(unsigned, __builtin_convertvector(v, bf2_t)); }
DI float rbf(float a) { return bflo(pk2(a, a)); }
DI f32x16 mfma32(bf16x8 a, bf16x8 b, f32x16 c) { return __builtin_amdgcn_mfma_f32_32x32x16_bf16(a, b, c, 0, 0, 0); }
DI f32x16 mfma32k8(s16x4 a, s16x4 b, f32x16 c) { return __builtin_amdgcn_mfma_f32_32x32x8bf16_1k(a, b, c, 0, 0, 0); }
DI f32x4 mfma16(u32x4 a, u32x4 b, f32x4 c) {
  return __builtin_amdgcn_mfma_f32_16x16x32_bf16(__builtin_bit_cast(bf16x8, a), __builtin_bit_cast(bf16x8, b), c, 0, 0, 0);
}
DI void glds16(const void* g, unsigned lds_base) {
  unsigned sv;
  asm volatile("s_mov_b32 %0, m0\n\ts_mov_b32 m0, %2\n\ts_nop 0\n\tglobal_load_lds_dwordx4 %1, off\n\ts_mov_b32 m0, %0"
               : "=&s"(sv) : "v"(g), "s"(lds_base) : "memory");
}
DI void glds4(const void* g, unsigned lds_base) {
  unsigned sv;
  asm volatile("s_mov_b32 %0, m0\n\ts_mov_b32 m0, %2\n\ts_nop 0\n\tglobal_load_lds_dword %1, off\n\ts_mov_b32 m0, %0"
               : "=&s"(sv) : "v"(g), "s"(lds_base) : "memory");
}
typedef short v4i16_t __attribute__((ext_vector_type(4)));
DI u32x2 tr_read(const LAS char* p) {
  return __builtin_bit_cast(u32x2, __builtin_amdgcn_ds_read_tr16_b64_v4i16((LAS v4i16_t*)p));
}
DI float xor32f(float v) {
  auto r = __builtin_amdgcn_permlane32_swap(__float_as_uint(v), __float_as_uint(v), false, false);
  return __uint_as_float(r[0]) + __uint_as_float(r[1]);
}

constexpr int K0_MOD = 108, K0_XH = 64, K0_QKV = 173, K0_RT = 689;
DI void retile_unit(const u16* __restrict__ src, int ld, int k0, int n0, char* dst, int f0, int f1, int f2, int f3,
                    LAS char* L, int lane) {
  u32x4 v[4];
#pragma unroll
  for (int i = 0; i < 4; ++i) v[i] = *(const u32x4*)(src + (size_t)(k0 + i * 8 + (lane >> 3)) * ld + n0 + (lane & 7) * 8);
#pragma unroll
  for (int i = 0; i < 4; ++i) *(LAS u32x4*)(L + (i * 8 + (lane >> 3)) * 144 + (lane & 7) * 16) = v[i];
  asm volatile("s_waitcnt lgkmcnt(0)" ::: "memory");
  const int q = lane >> 4, rr = (lane >> 2) & 3, pp = lane & 3;
  const int fi[4] = {f0, f1, f2, f3};
#pragma unroll
  for (int t = 0; t < 4; ++t) {
    u32x2 a = tr_read(L + (4 * q + rr) * 144 + t * 32 + pp * 8);
    u32x2 b = tr_read(L + (16 + 4 * q + rr) * 144 + t * 32 + pp * 8);
    u32x4 o = {a[0], a[1], b[0], b[1]};
    *(u32x4*)(dst + (size_t)fi[t] * 1024 + lane * 16) = o;
  }
}
__global__ __launch_bounds__(256) void k0_prep(const u16* __restrict__ x, const u16* __restrict__ wq, const u16* __restrict__ wk,
                                               const u16* __restrict__ wv, const u16* __restrict__ wo, const u16* __restrict__ w1,
                                               const u16* __restrict__ w2, const u16* __restrict__ c, const u16* __restrict__ wmod,
                                               char* __restrict__ ws) {
  __shared__ __attribute__((aligned(16))) char smem[64 * 33 * 4 > 4 * 32 * 144 ? 64 * 33 * 4 : 4 * 32 * 144];
  const int tid = threadIdx.x, lane = tid & 63, w = tid >> 6;
  int blk = blockIdx.x;
  if (blk < K0_MOD) {
    const int cq = tid & 3, rg = tid >> 2, n0 = blk * 32;
    u32x4 wv_[9];
    float sc[9];
#pragma unroll
    for (int i = 0; i < 9; ++i) wv_[i] = *(const u32x4*)(wmod + (size_t)(rg + 64 * i) * (6 * D_) + n0 + cq * 8);
#pragma unroll
    for (int i = 0; i < 9; ++i) {
      float cv = bf2f(c[rg + 64 * i]);
      sc[i] = rbf(cv / (1.f + __expf(-cv)));
    }
    float acc[8] = {0, 0, 0, 0, 0, 0, 0, 0};
#pragma unroll
    for (int i = 0; i < 9; ++i)
#pragma unroll
      for (int j = 0; j < 4; ++j) {
        acc[2 * j] += sc[i] * bflo(wv_[i][j]);
        acc[2 * j + 1] += sc[i] * bfhi(wv_[i][j]);
      }
    float* red = (float*)smem;
#pragma unroll
    for (int j = 0; j < 8; ++j) red[rg * 33 + cq * 8 + j] = acc[j];
    __syncthreads();
    if (tid < 32) {
      float s = 0.f;
#pragma unroll 16
      for (int r = 0; r < 64; ++r) s += red[r * 33 + tid];
      s = rbf(s);
      const int n = n0 + tid, part = n / D_;
      if (part == 1 || part == 4) s = 1.f + s;
      ((u16*)(ws + WS_MOD))[n] = (u16)(pk2(s, s) & 0xffffu);
    }
    return;
  }
  blk -= K0_MOD;
  if (blk < K0_XH) {
    const int rl = w * 8 + (lane >> 3), sub = lane & 7, row = blk * 32 + rl;
    u32x4 v[9];
#pragma unroll
    for (int i = 0; i < 9; ++i) v[i] = *(const u32x4*)(x + (size_t)row * D_ + (sub + 8 * i) * 8);
    float s = 0.f;
#pragma unroll
    for (int i = 0; i < 9; ++i)
#pragma unroll
      for (int j = 0; j < 4; ++j) s += bflo(v[i][j]) + bfhi(v[i][j]);
    s += __shfl_xor(s, 1); s += __shfl_xor(s, 2); s += __shfl_xor(s, 4);
    const float mean = s * (1.f / D_);
    float q = 0.f;
#pragma unroll
    for (int i = 0; i < 9; ++i)
#pragma unroll
      for (int j = 0; j < 4; ++j) {
        float a = bflo(v[i][j]) - mean, b2 = bfhi(v[i][j]) - mean;
        q += a * a + b2 * b2;
      }
    q += __shfl_xor(q, 1); q += __shfl_xor(q, 2); q += __shfl_xor(q, 4);
    const float rstd = 1.f / sqrtf(q * (1.f / D_) + LN_EPS);
#pragma unroll
    for (int i = 0; i < 9; ++i) {
      u32x4 o;
#pragma unroll
      for (int j = 0; j < 4; ++j) o[j] = pk2((bflo(v[i][j]) - mean) * rstd, (bfhi(v[i][j]) - mean) * rstd);
      const int p = sub + 8 * i;
      *(u32x4*)(ws + WS_XH + ((size_t)(blk * 36 + (p >> 1)) * 64 + (p & 1) * 32 + rl) * 16) = o;
    }
    return;
  }
  blk -= K0_XH;
  if (blk < K0_QKV) {
    const int idx = blk * 256 + tid;
    if (idx < 34 * 36 * 2 * 18) {
      const int d = idx % 18, sh = (idx / 18) % 72, hd = idx / (18 * 72);
      const u16* src; int ld;
      if (hd < 32) { src = wq + hd * DH + d; ld = D_; }
      else if (hd == 32) { src = wk + d; ld = DH; }
      else { src = wv + d; ld = DH; }
      unsigned e[8];
#pragma unroll
      for (int j = 0; j < 8; ++j) e[j] = src[(size_t)(sh * 8 + j) * ld];
      u32x4 o = {e[0] | (e[1] << 16), e[2] | (e[3] << 16), e[4] | (e[5] << 16), e[6] | (e[7] << 16)};
      *(u32x4*)(ws + WS_WQKV + (size_t)idx * 16) = o;
    }
    return;
  }
  blk -= K0_QKV;
  {
    int u = blk * 4 + w;
    LAS char* L = (LAS char*)smem + w * 32 * 144;
    if (u < 162) {
      const int s = u / 9, nb = u % 9;
      const int f = s * 36 + nb * 4;
      retile_unit(wo, D_, s * 32, nb * 64, ws + WS_WO, f, f + 1, f + 2, f + 3, L, lane);
      return;
    }
    u -= 162;
    if (u >= 2592) return;
    int fs, s, nb, ph; const u16* src; int ld, k0, n0;
    if (u < 1296) { ph = 0; s = u / 72; const int nbb = u % 72; fs = nbb / 9; nb = nbb % 9; src = w1; ld = FF; k0 = s * 32; n0 = nbb * 64; }
    else { u -= 1296; ph = 1; const int ks = u / 9; nb = u % 9; fs = ks / 18; s = ks % 18; src = w2; ld = D_; k0 = ks * 32; n0 = nb * 64; }
    int fi[4];
#pragma unroll
    for (int t = 0; t < 4; ++t) {
      const int tt = nb * 4 + t;
      fi[t] = (((fs * 12 + tt / 3) * 2 + ph) * 18 + s) * 3 + tt % 3;
    }
    retile_unit(src, ld, k0, n0, ws + WS_WF, fi[0], fi[1], fi[2], fi[3], L, lane);
  }
}

__global__ __launch_bounds__(576) void k1_qkv(char* __restrict__ ws) {
  __shared__ __attribute__((aligned(16))) char smem[36 * 1024];
  const int tid = threadIdx.x, lane = tid & 63, w = __builtin_amdgcn_readfirstlane(tid >> 6);
  const int T = blockIdx.x >> 2, g = blockIdx.x & 3;
  const int r = lane & 31, h = lane >> 5;
  const int hd = w < 8 ? g * 8 + w : 32 + g;
  const bool act = hd < 34;
  u32x4 xv[4], c1[4], sa[4];
#pragma unroll
  for (int i = 0; i < 4; ++i) {
    const int s = w * 4 + i;
    xv[i] = *(const u32x4*)(ws + WS_XH + ((size_t)(T * 36 + s) * 64 + lane) * 16);
    sa[i] = *(const u32x4*)(ws + WS_MOD + (s * 16 + h * 8) * 2);
    c1[i] = *(const u32x4*)(ws + WS_MOD + (D_ + s * 16 + h * 8) * 2);
  }
  constexpr int WIN = 18;
  u32x4 wf[WIN];
  const char* wp = ws + WS_WQKV + (size_t)(act ? hd : 0) * (36 * 576) + (h * 18 + (r < 18 ? r : 17)) * 16;
#pragma unroll
  for (int i = 0; i < WIN; ++i) wf[i] = *(const u32x4*)(wp + i * 576);
  SBAR();
#pragma unroll
  for (int i = 0; i < 4; ++i) {
    u32x4 o;
#pragma unroll
    for (int j = 0; j < 4; ++j)
      o[j] = pk2(rbf(bflo(xv[i][j]) * bflo(c1[i][j])) + bflo(sa[i][j]), rbf(bfhi(xv[i][j]) * bfhi(c1[i][j])) + bfhi(sa[i][j]));
    *(LAS u32x4*)((LAS char*)smem + ((w * 4 + i) * 64 + lane) * 16) = o;
  }
  __syncthreads();
  if (!act) return;
  f32x16 acc = {};
  const bool isv = hd == 33;
#pragma unroll
  for (int s = 0; s < 36; ++s) {
    const u32x4 cur = wf[s % WIN];
    if (s + WIN < 36) wf[s % WIN] = *(const u32x4*)(wp + (s + WIN) * 576);
    const u32x4 hf = *(const LAS u32x4*)((LAS char*)smem + (s * 64 + lane) * 16);
    const u32x4 a = isv ? hf : cur, b = isv ? cur : hf;
    acc = mfma32(__builtin_bit_cast(bf16x8, a), __builtin_bit_cast(bf16x8, b), acc);
  }
  if (isv) {
    if (r < 18) {
#pragma unroll
      for (int s = 0; s < 2; ++s) {
        u32x4 o = {pk2(acc[8 * s], acc[8 * s + 1]), pk2(acc[8 * s + 2], acc[8 * s + 3]), pk2(acc[8 * s + 4], acc[8 * s + 5]),
                   pk2(acc[8 * s + 6], acc[8 * s + 7])};
        *(u32x4*)(ws + WS_VF + (size_t)(((T * 2 + s) * 2 + h) * 18 + r) * 16) = o;
      }
    }
    return;
  }
  const float sc = hd < 32 ? QSCALE : 1.f;
  u32x4 o = {pk2(acc[0] * sc, acc[1] * sc), pk2(acc[2] * sc, acc[3] * sc), pk2(acc[4] * sc, acc[5] * sc), pk2(acc[6] * sc, acc[7] * sc)};
  const unsigned o2 = h == 0 ? pk2(acc[8] * sc, acc[9] * sc) : 0u;
  float n2 = bflo(o2) * bflo(o2) + bfhi(o2) * bfhi(o2);
#pragma unroll
  for (int j = 0; j < 4; ++j) n2 += bflo(o[j]) * bflo(o[j]) + bfhi(o[j]) * bfhi(o[j]);
  n2 = xor32f(n2);
  if (hd < 32) {
    const int b = T * 4 + (r >> 3), gb = hd >> 2, hl = hd & 3;
    const int l2 = h * 32 + hl * 8 + (r & 7);
    *(u32x4*)(ws + WS_QF + ((size_t)(b * 8 + gb) * 64 + l2) * 16) = o;
    const unsigned nb = (__float_as_uint(sqrtf(n2) * 1.0001f) + 0xffffu) >> 16;
    *(unsigned*)(ws + WS_QF2 + ((size_t)(b * 8 + gb) * 64 + l2) * 4) = h == 0 ? o2 : nb;
  } else {
    *(u32x4*)(ws + WS_KF + ((size_t)T * 64 + lane) * 16) = o;
    if (h == 0) *(unsigned*)(ws + WS_KF2 + (T * 32 + r) * 4) = o2;
#pragma unroll
    for (int m = 1; m < 32; m <<= 1) n2 = fmaxf(n2, __shfl_xor(n2, m));
    if (lane == 0) *(float*)(ws + WS_KMAX + T * 4) = n2;
  }
}

constexpr int L2_WO = 0, L2_PB = 110592, L2_OB = L2_PB + 20480, L2_Q = L2_OB + 9216, L2_Q2 = L2_Q + 8192, L2_ST = L2_Q2 + 2048,
              L2_FLAG = L2_ST + 512, L2_BYTES = L2_FLAG + 16;
#define EX2(v) __builtin_amdgcn_exp2f(v)

DI int ob_off(int k, int tok) {
  const int rr = k & 31;
  return (k >> 5) * 512 + ((rr >> 2) & 3) * 128 + tok * 16 + ((rr >> 4) * 4 + (rr & 3)) * 2;
}

__global__ __launch_bounds__(512, 2) void k2_attn(const u16* __restrict__ x, char* __restrict__ ws) {
  extern __shared__ __attribute__((aligned(16))) char smem[];
  LAS char* L = (LAS char*)smem;
  const unsigned lds0 = (unsigned)(uintptr_t)L;
  const int tid = threadIdx.x, lane = tid & 63, w = __builtin_amdgcn_readfirstlane(tid >> 6), b = blockIdx.x;
  const int r = lane & 31, h = lane >> 5;
  const int nt = w < 4 ? 5 : 4, t0 = w < 4 ? 9 * w : 9 * (w - 4) + 5;
  const int wo_base = w < 4 ? w * 15360 : 61440 + (w - 4) * 12288;
  glds16(ws + WS_QF + ((size_t)(b * 8 + w) * 64 + lane) * 16, lds0 + L2_Q + w * 1024);
  glds4(ws + WS_QF2 + ((size_t)(b * 8 + w) * 64 + lane) * 4, lds0 + L2_Q2 + w * 256);
  if (tid == 0) *(LAS unsigned*)(L + L2_FLAG) = 0u;
  float km = *(const float*)(ws + WS_KMAX + lane * 4);
  bf16x8 Kf[8], Vf[16];
  unsigned K2[8];
  const u32x4 vconst = r == 18 ? u32x4{0x3f803f80u, 0x3f803f80u, 0x3f803f80u, 0x3f803f80u} : u32x4{0u, 0u, 0u, 0u};
#pragma unroll
  for (int t = 0; t < 8; ++t) {
    const int T = w * 8 + t;
    Kf[t] = *(const bf16x8*)(ws + WS_KF + ((size_t)T * 64 + lane) * 16);
    K2[t] = *(const unsigned*)(ws + WS_KF2 + (T * 32 + r) * 4);
#pragma unroll
    for (int s = 0; s < 2; ++s) {
      u32x4 v = *(const u32x4*)(ws + WS_VF + (size_t)(((T * 2 + s) * 2 + h) * 18 + (r < 18 ? r : 17)) * 16);
      Vf[2 * t + s] = __builtin_bit_cast(bf16x8, r < 18 ? v : vconst);
    }
  }
#pragma unroll
  for (int m = 1; m < 64; m <<= 1) km = fmaxf(km, __shfl_xor(km, m));
  {
    const unsigned nk = ((__float_as_uint(sqrtf(km) * 1.0001f) + 0xffffu) >> 16) | 0x8000u;
#pragma unroll
    for (int t = 0; t < 8; ++t) K2[t] = h == 0 ? K2[t] : nk;
  }
  asm volatile("s_waitcnt vmcnt(0)" ::: "memory");
  const char* wsrc = ws + WS_WO + (size_t)t0 * 1024 + lane * 16;
#define WO_FILL(s)                                                                                      \
  do {                                                                                                  \
    const unsigned dst_ = lds0 + L2_WO + wo_base + ((s) % 3) * nt * 1024;                               \
    const char* src_ = wsrc + (size_t)(s) * 36 * 1024;                                                  \
    glds16(src_, dst_); glds16(src_ + 1024, dst_ + 1024); glds16(src_ + 2048, dst_ + 2048);             \
    glds16(src_ + 3072, dst_ + 3072);                                                                   \
    if (nt == 5) glds16(src_ + 4096, dst_ + 4096);                                                      \
  } while (0)
  WO_FILL(0); WO_FILL(1); WO_FILL(2);
  asm volatile("s_waitcnt lgkmcnt(0)" ::: "memory");
  __builtin_amdgcn_s_barrier();
  SBAR();

  f32x4 oacc[5];
#pragma unroll
  for (int i = 0; i < 5; ++i) oacc[i] = f32x4{0.f, 0.f, 0.f, 0.f};
  const f32x16 zero16 = {};
  f32x16 O = {}, Oold = {}, SA, SB;
  u32x4 Plo = {0u, 0u, 0u, 0u}, Phi = {0u, 0u, 0u, 0u};
  bf16x8 Q1 = *(const LAS bf16x8*)(L + L2_Q + lane * 16);
  unsigned q2 = *(const LAS unsigned*)(L + L2_Q2 + lane * 4);
#define KPAIR(t) __builtin_bit_cast(s16x4, u32x2{K2[(t) & ~1], K2[(t) | 1]})
#define QPAIR(t) __builtin_bit_cast(s16x4, ((t) & 1) ? u32x2{0u, q2} : u32x2{q2, 0u})
  SA = mfma32k8(KPAIR(0), QPAIR(0), zero16);
  SA = mfma32(Kf[0], Q1, SA);
#define EXP4(cur, i, P, j)                                                                              \
  {                                                                                                     \
    float e0 = EX2(cur[i]), e1 = EX2(cur[i + 1]), e2 = EX2(cur[i + 2]), e3 = EX2(cur[i + 3]);           \
    P[j] = pk2(e0, e1); P[j + 1] = pk2(e2, e3);                                                         \
    PIN(P[j]); PIN(P[j + 1]);                                                                           \
  }
#define TILE(t, cur, nxt)                                                                               \
  {                                                                                                     \
    nxt = mfma32k8(KPAIR(((t) + 1) & 7), QPAIR(((t) + 1) & 7), zero16);                                 \
    EXP4(cur, 0, Plo, 0); SBAR();                                                                       \
    nxt = mfma32(Kf[((t) + 1) & 7], Q1, nxt);                                                           \
    EXP4(cur, 4, Plo, 2); SBAR();                                                                       \
    if ((t) == 0) Oold = mfma32(Vf[15], __builtin_bit_cast(bf16x8, Phi), O);                            \
    else O = mfma32(Vf[(2 * (t) + 15) & 15], __builtin_bit_cast(bf16x8, Phi), O);                       \
    SBAR();                                                                                             \
    EXP4(cur, 8, Phi, 0); SBAR();                                                                       \
    O = mfma32(Vf[2 * (t)], __builtin_bit_cast(bf16x8, Plo), (t) == 0 ? zero16 : O);                    \
    EXP4(cur, 12, Phi, 2); SBAR();                                                                      \
  }
#define WRITE_PARTIAL()                                                                                 \
  {                                                                                                     \
    LAS char* pb = L + L2_PB + w * 2560 + r * 16;                                                       \
    *(LAS f32x4*)(pb + h * 512) = f32x4{Oold[0], Oold[1], Oold[2], Oold[3]};                            \
    *(LAS f32x4*)(pb + (2 + h) * 512) = f32x4{Oold[4], Oold[5], Oold[6], Oold[7]};                      \
    if (h == 0) *(LAS f32x4*)(pb + 4 * 512) = f32x4{Oold[8], Oold[9], Oold[10], Oold[11]};              \
  }
  const int rrow = 4 * w + (lane >> 4), rp = (lane & 15) < 10 ? (lane & 15) : 9;
  const LAS char* rsrc = L + L2_PB + (rp >> 1) * 512 + rrow * 16 + (rp & 1) * 8;
  f32x2 pv[8];
#define READ_PARTIALS()                                                                                 \
  { _Pragma("unroll") for (int i = 0; i < 8; ++i) pv[i] = *(const LAS f32x2*)(rsrc + i * 2560); }
#define REDUCE(gp)                                                                                      \
  {                                                                                                     \
    f32x2 s_ = ((pv[0] + pv[1]) + (pv[2] + pv[3])) + ((pv[4] + pv[5]) + (pv[6] + pv[7]));               \
    const float l_ = __builtin_bit_cast(float, __builtin_amdgcn_update_dpp(0, __builtin_bit_cast(int, s_[0]), 0x150 + 9, 0xf, 0xf, false)); \
    if (!(l_ >= 0x1p-64f)) *(LAS unsigned*)(L + L2_FLAG) = 1u;                                          \
    const float il_ = __builtin_amdgcn_rcpf(l_);                                                        \
    if ((lane & 15) < 9)                                                                                \
      *(LAS unsigned*)(L + L2_OB + ob_off(72 * (gp) + 18 * (rrow >> 3) + 2 * rp, rrow & 7)) = pk2(s_[0] * il_, s_[1] * il_); \
  }
#define OPROJ(s)                                                                                        \
  {                                                                                                     \
    const u32x4 of_ = *(const LAS u32x4*)(L + L2_OB + (s) * 512 + (lane >> 4) * 128 + (lane & 7) * 16); \
    const LAS char* wp_ = L + L2_WO + wo_base + ((s) % 3) * nt * 1024 + lane * 16;                      \
    _Pragma("unroll") for (int i = 0; i < 4; ++i) oacc[i] = mfma16(*(const LAS u32x4*)(wp_ + i * 1024), of_, oacc[i]); \
    if (nt == 5) oacc[4] = mfma16(*(const LAS u32x4*)(wp_ + 4096), of_, oacc[4]);                       \
  }
#define BAR_L() asm volatile("s_waitcnt lgkmcnt(0)\n\ts_barrier" ::: "memory")

  for (int g = 0; g < 8; ++g) {
    const int ka = (72 * (g - 1)) >> 5, kb = (72 * g) >> 5;
    TILE(0, SA, SB);
    if (g > 0) WRITE_PARTIAL();
    TILE(1, SB, SA);
    if (g > 0) { BAR_L(); READ_PARTIALS(); }
    TILE(2, SA, SB);
    if (g > 0) REDUCE(g - 1);
    TILE(3, SB, SA);
    if (g > 0) { asm volatile("s_waitcnt vmcnt(0)" ::: "memory"); BAR_L(); OPROJ(ka); }
    TILE(4, SA, SB);
    if (g > 0) OPROJ(ka + 1);
    TILE(5, SB, SA);
    if (g > 0 && kb - ka == 3) OPROJ(ka + 2);
    const int gn = g < 7 ? g + 1 : 7;
    bf16x8 Q1n = *(const LAS bf16x8*)(L + L2_Q + gn * 1024 + lane * 16);
    unsigned q2n = *(const LAS unsigned*)(L + L2_Q2 + gn * 256 + lane * 4);
    TILE(6, SA, SB);
    if (g > 0) { WO_FILL(ka + 3); WO_FILL(ka + 4); if (kb - ka == 3) WO_FILL(ka + 5); }
    Q1 = Q1n; q2 = q2n;
    TILE(7, SB, SA);
  }
  const int tok = lane & 7, q = lane >> 4;
  const int row = b * 8 + tok;
  u32x2 xv[5], gav[5], cbv[5], sbv[5];
#pragma unroll
  for (int i = 0; i < 5; ++i) {
    const int n = 16 * (t0 + (i < nt ? i : 0)) + 4 * q;
    xv[i] = *(const u32x2*)(x + (size_t)row * D_ + n);
    gav[i] = *(const u32x2*)(ws + WS_MOD + (2 * D_ + n) * 2);
    sbv[i] = *(const u32x2*)(ws + WS_MOD + (3 * D_ + n) * 2);
    cbv[i] = *(const u32x2*)(ws + WS_MOD + (4 * D_ + n) * 2);
  }
  Oold = mfma32(Vf[15], __builtin_bit_cast(bf16x8, Phi), O);
  WRITE_PARTIAL();
  BAR_L();
  READ_PARTIALS();
  REDUCE(7);
  asm volatile("s_waitcnt vmcnt(0)" ::: "memory");
  BAR_L();
  if (*(volatile LAS unsigned*)(L + L2_FLAG) != 0u) {
    const int rw = tid >> 1, hf = tid & 1;
    const int gb = rw >> 5, l32 = rw & 31;
    float qv[18];
    {
      const LAS u16* qa = (const LAS u16*)(L + L2_Q + gb * 1024);
      for (int j = 0; j < 8; ++j) {
        qv[(j & 3) + 8 * (j >> 2)] = bf2f(qa[l32 * 8 + j]);
        qv[(j & 3) + 8 * (j >> 2) + 4] = bf2f(qa[(32 + l32) * 8 + j]);
      }
      const unsigned q2v = *(const LAS unsigned*)(L + L2_Q2 + gb * 256 + l32 * 4);
      qv[16] = bflo(q2v); qv[17] = bfhi(q2v);
    }
    float mx = -INFINITY, l = 0.f, ov[18];
#pragma unroll
    for (int d = 0; d < 18; ++d) ov[d] = 0.f;
    for (int pass = 0; pass < 2; ++pass) {
      for (int key = hf * 1024; key < hf * 1024 + 1024; ++key) {
        const int T = key >> 5, kr = key & 31;
        const u32x4 ka_ = *(const u32x4*)(ws + WS_KF + ((size_t)T * 64 + kr) * 16);
        const u32x4 kb_ = *(const u32x4*)(ws + WS_KF + ((size_t)T * 64 + 32 + kr) * 16);
        const unsigned kc_ = *(const unsigned*)(ws + WS_KF2 + (T * 32 + kr) * 4);
        float s = qv[16] * bflo(kc_) + qv[17] * bfhi(kc_);
#pragma unroll
        for (int j = 0; j < 4; ++j) {
          const int d0 = (2 * j & 3) + 8 * (j >> 1);
          s += qv[d0] * bflo(ka_[j]) + qv[d0 + 1] * bfhi(ka_[j]) + qv[d0 + 4] * bflo(kb_[j]) + qv[d0 + 5] * bfhi(kb_[j]);
        }
        if (pass == 0) mx = fmaxf(mx, s);
        else {
          const float p = rbf(EX2(s - mx));
          l += p;
          const int st = kr >> 4, hh = (kr >> 2) & 1, j = ((kr >> 3) & 1) * 4 + (kr & 3);
          const u16* vp = (const u16*)(ws + WS_VF + (size_t)(((T * 2 + st) * 2 + hh) * 18) * 16) + j;
#pragma unroll
          for (int d = 0; d < 18; ++d) ov[d] += p * bf2f(vp[d * 8]);
        }
      }
      if (pass == 0) mx = fmaxf(mx, __shfl_xor(mx, 1));
    }
    l += __shfl_xor(l, 1);
    const float il = 1.f / l;
#pragma unroll
    for (int d = 0; d < 18; ++d) ov[d] = (ov[d] + __shfl_xor(ov[d], 1)) * il;
    if (hf == 0) {
#pragma unroll
      for (int d = 0; d < 18; d += 2)
        *(LAS unsigned*)(L + L2_OB + ob_off(72 * gb + 18 * (l32 >> 3) + d, l32 & 7)) = pk2(ov[d], ov[d + 1]);
    }
    __syncthreads();
#pragma unroll
    for (int i = 0; i < 5; ++i) oacc[i] = f32x4{0.f, 0.f, 0.f, 0.f};
    for (int s = 0; s < 15; ++s) {
      const u32x4 of_ = *(const LAS u32x4*)(L + L2_OB + s * 512 + (lane >> 4) * 128 + (lane & 7) * 16);
#pragma unroll
      for (int i = 0; i < 5; ++i)
        if (i < nt) oacc[i] = mfma16(*(const u32x4*)(wsrc + (size_t)s * 36 * 1024 + i * 1024), of_, oacc[i]);
    }
  }
  OPROJ(15); OPROJ(16); OPROJ(17);
  float x1[5][4];
  float s1 = 0.f;
#pragma unroll
  for (int i = 0; i < 5; ++i) {
    x1[i][0] = rbf(bflo(xv[i][0]) + rbf(bflo(gav[i][0]) * rbf(oacc[i][0])));
    x1[i][1] = rbf(bfhi(xv[i][0]) + rbf(bfhi(gav[i][0]) * rbf(oacc[i][1])));
    x1[i][2] = rbf(bflo(xv[i][1]) + rbf(bflo(gav[i][1]) * rbf(oacc[i][2])));
    x1[i][3] = rbf(bfhi(xv[i][1]) + rbf(bfhi(gav[i][1]) * rbf(oacc[i][3])));
    if (i < nt) s1 += (x1[i][0] + x1[i][1]) + (x1[i][2] + x1[i][3]);
  }
  s1 += __shfl_xor(s1, 16);
  s1 = xor32f(s1);
  const float mw = s1 / (float)(16 * nt);
  float m2 = 0.f;
#pragma unroll
  for (int i = 0; i < 5; ++i)
    if (i < nt) {
#pragma unroll
      for (int j = 0; j < 4; ++j) m2 += (x1[i][j] - mw) * (x1[i][j] - mw);
    }
  m2 += __shfl_xor(m2, 16);
  m2 = xor32f(m2);
  if (lane < 8) *(LAS f32x2*)(L + L2_ST + (tok * 8 + w) * 8) = f32x2{mw, m2};
  if ((lane & 8) == 0) {
#pragma unroll
    for (int i = 0; i < 5; ++i)
      if (i < nt) *(u32x2*)(ws + WS_X1 + ((size_t)row * D_ + 16 * (t0 + i) + 4 * q) * 2) = u32x2{pk2(x1[i][0], x1[i][1]), pk2(x1[i][2], x1[i][3])};
  }
  BAR_L();
  float mean = 0.f, M2 = 0.f;
  {
    f32x4 st[4];
#pragma unroll
    for (int i = 0; i < 4; ++i) st[i] = *(const LAS f32x4*)(L + L2_ST + tok * 64 + i * 16);
    mean = ((st[0][0] + st[0][2] + st[1][0] + st[1][2]) * 80.f + (st[2][0] + st[2][2] + st[3][0] + st[3][2]) * 64.f) * (1.f / D_);
#pragma unroll
    for (int i = 0; i < 4; ++i) {
      const float nw = i < 2 ? 80.f : 64.f, da = st[i][0] - mean, db = st[i][2] - mean;
      M2 += st[i][1] + st[i][3] + nw * (da * da + db * db);
    }
  }
  const float rstd = 1.f / sqrtf(M2 * (1.f / D_) + LN_EPS);
  if ((lane & 8) == 0) {
#pragma unroll
    for (int i = 0; i < 5; ++i)
      if (i < nt) {
        const int t = t0 + i;
        const float h0 = rbf(rbf((x1[i][0] - mean) * rstd) * bflo(cbv[i][0])) + bflo(sbv[i][0]);
        const float h1 = rbf(rbf((x1[i][1] - mean) * rstd) * bfhi(cbv[i][0])) + bfhi(sbv[i][0]);
        const float h2 = rbf(rbf((x1[i][2] - mean) * rstd) * bflo(cbv[i][1])) + bflo(sbv[i][1]);
        const float h3 = rbf(rbf((x1[i][3] - mean) * rstd) * bfhi(cbv[i][1])) + bfhi(sbv[i][1]);
        *(u32x2*)(ws + WS_H2 + ((size_t)((b >> 1) * 18 + (t >> 1)) * 64 + q * 16 + (b & 1) * 8 + tok) * 16 + (t & 1) * 8) =
            u32x2{pk2(h0, h1), pk2(h2, h3)};
      }
  }
}

constexpr int L3_A = 0, L3_H = 73728, L3_BYTES = 147456;
__global__ __launch_bounds__(768, 3) void k3_ffn(char* __restrict__ ws) {
  extern __shared__ __attribute__((aligned(16))) char smem[];
  LAS char* L = (LAS char*)smem;
  const unsigned lds0 = (unsigned)(uintptr_t)L;
  const int tid = threadIdx.x, lane = tid & 63, w = __builtin_amdgcn_readfirstlane(tid >> 6);
  const int fs = blockIdx.x & 7, m = blockIdx.x >> 3;
#pragma unroll
  for (int i = 0; i < 6; ++i)
    glds16(ws + WS_H2 + (size_t)m * 73728 + (size_t)(w * 6 + i) * 1024 + lane * 16, lds0 + L3_A + (w * 6 + i) * 1024);
  constexpr int WIN = 12;
  u32x4 wf[WIN];
  const char* wp = ws + WS_WF + (size_t)(fs * 12 + w) * (108 * 1024) + lane * 16;
#pragma unroll
  for (int i = 0; i < WIN; ++i) wf[i] = *(const u32x4*)(wp + i * 1024);
  SBAR();
  asm volatile("s_waitcnt vmcnt(12)\n\ts_barrier" ::: "memory");
  SBAR();
  f32x4 acc[3][4];
#pragma unroll
  for (int ph = 0; ph < 2; ++ph) {
#pragma unroll
    for (int i = 0; i < 3; ++i)
#pragma unroll
      for (int j = 0; j < 4; ++j) acc[i][j] = f32x4{0.f, 0.f, 0.f, 0.f};
    const LAS char* ab = L + (ph ? L3_H : L3_A) + lane * 16;
#pragma unroll
    for (int s = 0; s < 18; ++s) {
      u32x4 af[4];
#pragma unroll
      for (int j = 0; j < 4; ++j) af[j] = *(const LAS u32x4*)(ab + (j * 18 + s) * 1024);
#pragma unroll
      for (int i = 0; i < 3; ++i) {
        const int fi = (ph * 18 + s) * 3 + i;
        const u32x4 cur = wf[fi % WIN];
        if (fi + WIN < 108) wf[fi % WIN] = *(const u32x4*)(wp + (fi + WIN) * 1024);
#pragma unroll
        for (int j = 0; j < 4; ++j) acc[i][j] = mfma16(cur, af[j], acc[i][j]);
      }
      SBAR();
    }
    if (ph == 0) {
#pragma unroll
      for (int i = 0; i < 3; ++i) {
        const int t = w * 3 + i;
#pragma unroll
        for (int j = 0; j < 4; ++j) {
          const u32x2 o = {pk2(fmaxf(acc[i][j][0], 0.f), fmaxf(acc[i][j][1], 0.f)), pk2(fmaxf(acc[i][j][2], 0.f), fmaxf(acc[i][j][3], 0.f))};
          *(LAS u32x2*)(L + L3_H + (j * 18 + (t >> 1)) * 1024 + lane * 16 + (t & 1) * 8) = o;
        }
      }
      asm volatile("s_waitcnt lgkmcnt(0)\n\ts_barrier" ::: "memory");
      SBAR();
    }
  }
#pragma unroll
  for (int i = 0; i < 3; ++i)
#pragma unroll
    for (int jp = 0; jp < 2; ++jp) {
      const u32x4 o = {pk2(acc[i][2 * jp][0], acc[i][2 * jp][1]), pk2(acc[i][2 * jp][2], acc[i][2 * jp][3]),
                       pk2(acc[i][2 * jp + 1][0], acc[i][2 * jp + 1][1]), pk2(acc[i][2 * jp + 1][2], acc[i][2 * jp + 1][3])};
      *(u32x4*)(ws + WS_PART + ((((size_t)(m * 8 + fs) * 36 + w * 3 + i) * 2 + jp) * 64 + lane) * 16) = o;
    }
}

__global__ __launch_bounds__(256) void k4_reduce(const char* __restrict__ ws, u16* __restrict__ out) {
  const int u = blockIdx.x * 256 + threadIdx.x;
  const int lane = u & 63, jp = (u >> 6) & 1, t = (u >> 7) % 36, m = (u >> 7) / 36;
  u32x4 pv[8];
#pragma unroll
  for (int f = 0; f < 8; ++f) pv[f] = *(const u32x4*)(ws + WS_PART + ((((size_t)(m * 8 + f) * 36 + t) * 2 + jp) * 64 + lane) * 16);
  const int n = 16 * t + 4 * (lane >> 4), row = m * 64 + jp * 32 + (lane & 15);
  const u32x2 g = *(const u32x2*)(ws + WS_MOD + (5 * D_ + n) * 2);
  const u32x2 xa = *(const u32x2*)(ws + WS_X1 + ((size_t)row * D_ + n) * 2);
  const u32x2 xb = *(const u32x2*)(ws + WS_X1 + ((size_t)(row + 16) * D_ + n) * 2);
  float s[8] = {0, 0, 0, 0, 0, 0, 0, 0};
#pragma unroll
  for (int f = 0; f < 8; ++f)
#pragma unroll
    for (int j = 0; j < 4; ++j) { s[2 * j] += bflo(pv[f][j]); s[2 * j + 1] += bfhi(pv[f][j]); }
  const float g0 = bflo(g[0]), g1 = bfhi(g[0]), g2 = bflo(g[1]), g3 = bfhi(g[1]);
  *(u32x2*)(out + (size_t)row * D_ + n) = u32x2{pk2(bflo(xa[0]) + rbf(g0 * rbf(s[0])), bfhi(xa[0]) + rbf(g1 * rbf(s[1]))),
                                                 pk2(bflo(xa[1]) + rbf(g2 * rbf(s[2])), bfhi(xa[1]) + rbf(g3 * rbf(s[3])))};
  *(u32x2*)(out + (size_t)(row + 16) * D_ + n) = u32x2{pk2(bflo(xb[0]) + rbf(g0 * rbf(s[4])), bfhi(xb[0]) + rbf(g1 * rbf(s[5]))),
                                                        pk2(bflo(xb[1]) + rbf(g2 * rbf(s[6])), bfhi(xb[1]) + rbf(g3 * rbf(s[7])))};
}

extern "C" void kernel_launch(void* const* d_in, const int* in_sizes, int n_in, void* d_out, int out_size, void* d_ws,
                              size_t ws_size, hipStream_t stream) {
  const u16* x = (const u16*)d_in[0];
  const u16* wq = (const u16*)d_in[1];
  const u16* wk = (const u16*)d_in[2];
  const u16* wv = (const u16*)d_in[3];
  const u16* wo = (const u16*)d_in[4];
  const u16* w1 = (const u16*)d_in[6];
  const u16* w2 = (const u16*)d_in[7];
  const u16* c = (const u16*)d_in[8];
  const u16* wmod = (const u16*)d_in[9];
  char* ws = (char*)d_ws;
  (void)hipFuncSetAttribute((const void*)k2_attn, hipFuncAttributeMaxDynamicSharedMemorySize, L2_BYTES);
  (void)hipFuncSetAttribute((const void*)k3_ffn, hipFuncAttributeMaxDynamicSharedMemorySize, L3_BYTES);
  k0_prep<<<K0_MOD + K0_XH + K0_QKV + K0_RT, 256, 0, stream>>>(x, wq, wk, wv, wo, w1, w2, c, wmod, ws);
  k1_qkv<<<256, 576, 0, stream>>>(ws);
  k2_attn<<<256, 512, L2_BYTES, stream>>>(x, ws);
  k3_ffn<<<256, 768, L3_BYTES, stream>>>(ws);
  k4_reduce<<<576, 256, 0, stream>>>(ws, (u16*)d_out);
}
```

```cpp
#include <hip/hip_runtime.h>
#include <hip/hip_bf16.h>
#include <stdint.h>
#include <type_traits>

typedef unsigned short u16;
typedef short bf16x8 __attribute__((ext_vector_type(8)));
typedef short s16x4 __attribute__((ext_vector_type(4)));
typedef float f32x2 __attribute__((ext_vector_type(2)));
typedef float f32x4 __attribute__((ext_vector_type(4)));
typedef float f32x16 __attribute__((ext_vector_type(16)));
typedef unsigned u32x2 __attribute__((ext_vector_type(2)));
typedef unsigned u32x4 __attribute__((ext_vector_type(4)));
typedef __bf16 bf2_t __attribute__((ext_vector_type(2)));
#define LAS __attribute__((address_space(3)))
#define DI __device__ __forceinline__
#define SBAR() __builtin_amdgcn_sched_barrier(0)
#define PIN(x) asm volatile("" : "+v"(x))
#define EX2(v) __builtin_amdgcn_exp2f(v)

constexpr int S_ = 2048, D_ = 576, NH = 32, DH = 18, FF = 4608;
constexpr float QSCALE = 0.23570226039551584f * 1.4426950408889634f;
constexpr float LN_EPS = 1e-5f;

constexpr size_t WS_MOD = 0;
constexpr size_t WS_KMAX = 8192;
constexpr size_t WS_EPOCH = 8704;
constexpr size_t WS_ACK = 9216;
constexpr size_t WS_GRAN = 10240;
constexpr size_t WS_CNT = 16384;
constexpr size_t WS_WQ = 32768;
constexpr size_t WS_WO = WS_WQ + 663552;
constexpr size_t WS_WF = WS_WO + 663552;
constexpr size_t WS_QF = WS_WF + 10616832;
constexpr size_t WS_QF2 = WS_QF + 2097152;
constexpr size_t WS_KF = WS_QF2 + 524288;
constexpr size_t WS_KF2 = WS_KF + 65536;
constexpr size_t WS_VF = WS_KF2 + 8192;
constexpr size_t WS_X1 = WS_VF + 81920;
constexpr size_t WS_H2 = WS_X1 + 2359296;
constexpr size_t WS_PART = WS_H2 + 2359296;
constexpr size_t WS_END = WS_PART + 18874368;

#define STAMP_INIT()
#define STAMP(kern, i)
#define STAMP_FLUSH(kern)
DI float bf2f(unsigned b) { return __uint_as_float(b << 16); }
DI float bflo(unsigned v) { return __uint_as_float(v << 16); }
DI float bfhi(unsigned v) { return __uint_as_float(v & 0xffff0000u); }
DI unsigned pk2(float a, float b) { f32x2 v = {a, b}; return __builtin_bit_cast(unsigned, __builtin_convertvector(v, bf2_t)); }
DI float rbf(float a) { return bflo(pk2(a, a)); }
DI f32x16 mfma32(bf16x8 a, bf16x8 b, f32x16 c) { return __builtin_amdgcn_mfma_f32_32x32x16_bf16(a, b, c, 0, 0, 0); }
DI f32x16 mfma32k8(s16x4 a, s16x4 b, f32x16 c) { return __builtin_amdgcn_mfma_f32_32x32x8bf16_1k(a, b, c, 0, 0, 0); }
DI f32x4 mfma16(u32x4 a, u32x4 b, f32x4 c) {
  return __builtin_amdgcn_mfma_f32_16x16x32_bf16(__builtin_bit_cast(bf16x8, a), __builtin_bit_cast(bf16x8, b), c, 0, 0, 0);
}
DI void glds16(const void* g, unsigned lds_base) {
  unsigned sv;
  asm volatile("s_mov_b32 %0, m0\n\ts_mov_b32 m0, %2\n\ts_nop 0\n\tglobal_load_lds_dwordx4 %1, off\n\ts_mov_b32 m0, %0"
               : "=&s"(sv) : "v"(g), "s"(lds_base) : "memory");
}
DI void glds4(const void* g, unsigned lds_base) {
  unsigned sv;
  asm volatile("s_mov_b32 %0, m0\n\ts_mov_b32 m0, %2\n\ts_nop 0\n\tglobal_load_lds_dword %1, off\n\ts_mov_b32 m0, %0"
               : "=&s"(sv) : "v"(g), "s"(lds_base) : "memory");
}
template <int OFF> DI void glds16s(const void* sbase, unsigned voff, unsigned lds_base) {
  unsigned sv;
  asm volatile("s_mov_b32 %0, m0\n\ts_mov_b32 m0, %3\n\ts_nop 0\n\tglobal_load_lds_dwordx4 %1, %2 offset:%4\n\ts_mov_b32 m0, %0"
               : "=&s"(sv) : "v"(voff), "s"(sbase), "s"(lds_base), "i"(OFF) : "memory");
}
template <int CTRL> DI float dppf(float old, float v) {
  return __builtin_bit_cast(float, __builtin_amdgcn_update_dpp(__builtin_bit_cast(int, old), __builtin_bit_cast(int, v), CTRL, 0xf, 0xf, false));
}
DI float wave_sum(float v) {
  v += dppf<0x111>(0.f, v); v += dppf<0x112>(0.f, v); v += dppf<0x114>(0.f, v); v += dppf<0x118>(0.f, v);
  const float a = __builtin_bit_cast(float, __builtin_amdgcn_readlane(__builtin_bit_cast(int, v), 15));
  const float b = __builtin_bit_cast(float, __builtin_amdgcn_readlane(__builtin_bit_cast(int, v), 31));
  const float c = __builtin_bit_cast(float, __builtin_amdgcn_readlane(__builtin_bit_cast(int, v), 47));
  const float d = __builtin_bit_cast(float, __builtin_amdgcn_readlane(__builtin_bit_cast(int, v), 63));
  return (a + b) + (c + d);
}
DI float wave_max_nonneg(float v) {
  v = fmaxf(v, dppf<0x111>(0.f, v)); v = fmaxf(v, dppf<0x112>(0.f, v)); v = fmaxf(v, dppf<0x114>(0.f, v)); v = fmaxf(v, dppf<0x118>(0.f, v));
  const float a = __builtin_bit_cast(float, __builtin_amdgcn_readlane(__builtin_bit_cast(int, v), 15));
  const float b = __builtin_bit_cast(float, __builtin_amdgcn_readlane(__builtin_bit_cast(int, v), 31));
  const float c = __builtin_bit_cast(float, __builtin_amdgcn_readlane(__builtin_bit_cast(int, v), 47));
  const float d = __builtin_bit_cast(float, __builtin_amdgcn_readlane(__builtin_bit_cast(int, v), 63));
  return fmaxf(fmaxf(a, b), fmaxf(c, d));
}
typedef short v4i16_t __attribute__((ext_vector_type(4)));
DI u32x2 tr_read(const LAS char* p) {
  return __builtin_bit_cast(u32x2, __builtin_amdgcn_ds_read_tr16_b64_v4i16((LAS v4i16_t*)p));
}
DI float xor32f(float v) {
  auto r = __builtin_amdgcn_permlane32_swap(__float_as_uint(v), __float_as_uint(v), false, false);
  return __uint_as_float(r[0]) + __uint_as_float(r[1]);
}

#define GAS __attribute__((address_space(1)))
#define RLX_AGENT __ATOMIC_RELAXED, __HIP_MEMORY_SCOPE_AGENT
#define PRIO(n) __builtin_amdgcn_s_setprio(n)
#define BAR_L() asm volatile("s_waitcnt lgkmcnt(0)\n\ts_barrier" ::: "memory")
constexpr int AUX_SC1 = 16;
constexpr unsigned SPIN_CAP = 1u << 22;
DI int wf_index(int ph, int fs, int s, int t) {
  if (ph == 0) return (fs * 12 + t / 3) * 108 + s * 3 + t % 3;
  int rho = t - 9 * (fs >> 1);
  if (rho < 0) rho += 36;
  return (fs * 12 + rho % 12) * 108 + 54 + (2 - rho / 12) * 18 + s;
}
constexpr int L1_H = 0, L1_OUT = 36864, L1_RT = L1_OUT + 2 * 25088, L1_M = L1_RT + 3 * 4608, L1_RED = L1_M + 4608,
              L1_FLG = L1_RED + 1536, L1_DUMMY = L1_FLG + 32, L1_BYTES = L1_DUMMY + 256;
#define ADD_ROR(v, n) asm volatile("s_nop 1\n\tv_add_f32_dpp %0, %0, %0 row_ror:" #n " row_mask:0xf bank_mask:0xf" : "+v"(v))
DI void lds_arrive(LAS char* p, int lane) {
  if (lane == 0) __hip_atomic_fetch_add((LAS unsigned*)p, 1u, __ATOMIC_RELAXED, __HIP_MEMORY_SCOPE_WORKGROUP);
}
DI void lds_wait(LAS char* p, unsigned n) {
  for (unsigned spins = 0; spins < SPIN_CAP; ++spins)
    { if (__builtin_amdgcn_readfirstlane(*(volatile LAS unsigned*)p) >= n) break; __builtin_amdgcn_s_sleep(1); }
  asm volatile("" ::: "memory");
}
__global__ __launch_bounds__(768, 3) void k1_qkv(const u16* __restrict__ x, const u16* __restrict__ wq, const u16* __restrict__ wk,
                                                 const u16* __restrict__ wv, const u16* __restrict__ wo, const u16* __restrict__ c,
                                                 const u16* __restrict__ wmod, char* __restrict__ ws) {
  extern __shared__ __attribute__((aligned(16))) char smem[];
  LAS char* L = (LAS char*)smem;
  const unsigned lds0 = (unsigned)(uintptr_t)L;
  const int tid = threadIdx.x, lane = tid & 63, w = __builtin_amdgcn_readfirstlane(tid >> 6), b = blockIdx.x;
  STAMP_INIT();
  STAMP(1, 0);
  {
    unsigned long long pc;
    int len;
    asm volatile("s_getpc_b64 %0\nadaln_code_begin:\n\ts_mov_b32 %1, adaln_code_end-adaln_code_begin" : "=s"(pc), "=s"(len));
    const int off = tid * 128;
    glds4((const char*)pc + (off < len ? off : 0), lds0 + L1_DUMMY);
  }
  const int xcd = b & 7, jx = b >> 3;
  const int T = (xcd >> 1) * 16 + (jx >> 1), g = (xcd & 1) * 2 + (jx & 1);
  const int q = lane >> 4, n16 = lane & 15;
  const unsigned l16 = lane * 16;
  const LAS char* hb0 = L + L1_H + q * 256 + (n16 ^ q) * 16;
  const LAS char* hb1 = L + L1_H + q * 256 + (n16 ^ q ^ 12) * 16;
#define HFRAG(mt, ks) (*(const LAS u32x4*)((((ks) & 1) ? hb1 : hb0) + (mt) * 18432 + (ks) * 1024))
  unsigned tag = 0;
  if (w < 9) {
    u32x4 xv[4];
    unsigned xe[4];
    {
      const char* xb = (const char*)x + (size_t)(T * 32 + (w & 7) * 4) * 1152;
#pragma unroll
      for (int i = 0; i < 4; ++i) { xv[i] = *(const u32x4*)(xb + i * 1152 + l16); xe[i] = *(const u16*)(xb + i * 1152 + 1024 + lane * 2); }
    }
    SBAR();
    STAMP(1, 1);
    asm volatile("s_barrier" ::: "memory");
    STAMP(1, 2);
    SBAR();
    f32x2 xh[4][4];
    float xh8[4];
    if (w < 8) {
#pragma unroll
      for (int i = 0; i < 4; ++i) {
#pragma unroll
        for (int j = 0; j < 4; ++j) xh[i][j] = f32x2{bflo(xv[i][j]), bfhi(xv[i][j])};
        xh8[i] = bflo(xe[i]);
        const f32x2 s2 = (xh[i][0] + xh[i][1]) + (xh[i][2] + xh[i][3]);
        const float mean = wave_sum(s2[0] + s2[1] + xh8[i]) * (1.f / D_);
        xh8[i] -= mean;
        f32x2 q2 = {xh8[i] * xh8[i], 0.f};
#pragma unroll
        for (int j = 0; j < 4; ++j) { xh[i][j] -= mean; q2 = __builtin_elementwise_fma(xh[i][j], xh[i][j], q2); }
        const float rstd = __builtin_amdgcn_rsqf(wave_sum(q2[0] + q2[1]) * (1.f / D_) + LN_EPS);
        xh8[i] *= rstd;
#pragma unroll
        for (int j = 0; j < 4; ++j) xh[i][j] *= rstd;
      }
#pragma unroll
      for (int i = 0; i < 4; ++i) { PIN(xh8[i]); PIN(xh[i][0]); PIN(xh[i][1]); PIN(xh[i][2]); PIN(xh[i][3]); }
    }
    STAMP(1, 5);
    lds_wait(L + L1_FLG, 1);
    STAMP(1, 6);
    u32x4 wf[18];
    {
      const __amdgpu_buffer_rsrc_t rq = __builtin_amdgcn_make_buffer_rsrc(ws + WS_WQ + (size_t)(g * 9 + w) * 18432, 0, 18432, 0x00020000);
#pragma unroll
      for (int s = 0; s < 18; ++s) wf[s] = __builtin_amdgcn_raw_buffer_load_b128(rq, l16, s * 1024, AUX_SC1);
    }
    SBAR();
    lds_wait(L + L1_FLG + 16, 1);
    STAMP(1, 7);
    if (w < 8) {
      const LAS char* mp = L + L1_M;
      const f32x4 s0 = *(const LAS f32x4*)(mp + lane * 32), s1 = *(const LAS f32x4*)(mp + lane * 32 + 16);
      const f32x4 c0 = *(const LAS f32x4*)(mp + 2304 + lane * 32), c1 = *(const LAS f32x4*)(mp + 2304 + lane * 32 + 16);
      const float s8 = *(const LAS float*)(mp + 2048 + lane * 4), c8 = *(const LAS float*)(mp + 2304 + 2048 + lane * 4);
      const int sz = (lane & 3) | ((lane >> 2) & 1) * 12, sz8 = ((lane >> 3) & 3) | (lane >> 5) * 12;
      LAS char* hp = L + L1_H + (w >> 2) * 18432;
#pragma unroll
      for (int i = 0; i < 4; ++i) {
        const int tok = (w & 3) * 4 + i;
        const u32x4 o = {pk2(fmaf(xh[i][0][0], c0[0], s0[0]), fmaf(xh[i][0][1], c0[1], s0[1])),
                         pk2(fmaf(xh[i][1][0], c0[2], s0[2]), fmaf(xh[i][1][1], c0[3], s0[3])),
                         pk2(fmaf(xh[i][2][0], c1[0], s1[0]), fmaf(xh[i][2][1], c1[1], s1[1])),
                         pk2(fmaf(xh[i][3][0], c1[2], s1[2]), fmaf(xh[i][3][1], c1[3], s1[3]))};
        *(LAS u32x4*)(hp + lane * 256 + (tok ^ sz) * 16) = o;
        *(LAS u16*)(hp + 16384 + (lane >> 3) * 256 + (tok ^ sz8) * 16 + (lane & 7) * 2) = (u16)pk2(fmaf(xh8[i], c8, s8), 0.f);
      }
    }
    lds_arrive(L + L1_FLG + 8, lane);
    lds_wait(L + L1_FLG + 8, 9);
    STAMP(1, 8);
    f32x4 acc[2] = {};
    u32x4 hf[10];
#pragma unroll
    for (int i = 0; i < 10; ++i) hf[i] = HFRAG(i & 1, i >> 1);
    SBAR();
#pragma unroll
    for (int i = 0; i < 36; ++i) {
      acc[i & 1] = mfma16(wf[i >> 1], hf[i % 10], acc[i & 1]);
      SBAR();
      if (i + 10 < 36) hf[i % 10] = HFRAG(i & 1, (i + 10) >> 1);
      SBAR();
    }
#pragma unroll
    for (int mt = 0; mt < 2; ++mt) *(LAS f32x4*)(L + L1_OUT + (mt * 16 + n16) * 784 + w * 64 + q * 16) = acc[mt];
    lds_arrive(L + L1_FLG + 12, lane);
  } else {
    const int sw = w - 9;
#define LOAD_TAG() tag = __hip_atomic_load((GAS unsigned*)(ws + WS_EPOCH), RLX_AGENT)
#define MAKE_TAG() tag += 0x9E3779B9u
    auto gemv = [&](int pidx, auto urgent) {
      PRIO(3);
      u32x4 mv[12];
      float sc[3];
      const u16* mp = wmod + (size_t)(16 * sw + (lane >> 2)) * (6 * D_) + pidx * 32 + (lane & 3) * 8;
      unsigned cv[3];
      if (urgent.value) { LOAD_TAG(); SBAR(); }
#pragma unroll
      for (int i = 0; i < 3; ++i) cv[i] = c[16 * sw + (lane >> 2) + 48 * (4 * i + (lane & 3))];
#pragma unroll
      for (int i = 0; i < 12; ++i) mv[i] = *(const u32x4*)(mp + (size_t)i * 48 * (6 * D_));
      SBAR();
      if (urgent.value) { STAMP(1, 1); BAR_L(); STAMP(1, 2); SBAR(); MAKE_TAG(); }
#pragma unroll
      for (int i = 0; i < 3; ++i) {
        const float cf = bf2f(cv[i]);
        sc[i] = rbf(cf * __builtin_amdgcn_rcpf(1.f + EX2(-1.4426950408889634f * cf)));
      }
      if (urgent.value && tid == 704) __hip_atomic_store((GAS unsigned*)(ws + WS_ACK + b * 4), tag, RLX_AGENT);
      f32x2 acc[4] = {};
#pragma unroll
      for (int i = 0; i < 12; ++i) {
        const int ii = i & 3;
        const int sb_ = __builtin_bit_cast(int, sc[i >> 2]);
        const float sv = __builtin_bit_cast(float, ii == 0 ? __builtin_amdgcn_mov_dpp(sb_, 0x00, 0xf, 0xf, true)
                                                 : ii == 1 ? __builtin_amdgcn_mov_dpp(sb_, 0x55, 0xf, 0xf, true)
                                                 : ii == 2 ? __builtin_amdgcn_mov_dpp(sb_, 0xaa, 0xf, 0xf, true)
                                                           : __builtin_amdgcn_mov_dpp(sb_, 0xff, 0xf, 0xf, true));
#pragma unroll
        for (int j = 0; j < 4; ++j) acc[j] = __builtin_elementwise_fma(f32x2{sv, sv}, f32x2{bflo(mv[i][j]), bfhi(mv[i][j])}, acc[j]);
        SBAR();
      }
      asm volatile("s_nop 1\n\t"
                   "v_add_f32_dpp %0, %0, %0 row_ror:8 row_mask:0xf bank_mask:0xf\n\tv_add_f32_dpp %1, %1, %1 row_ror:8 row_mask:0xf bank_mask:0xf\n\t"
                   "v_add_f32_dpp %2, %2, %2 row_ror:8 row_mask:0xf bank_mask:0xf\n\tv_add_f32_dpp %3, %3, %3 row_ror:8 row_mask:0xf bank_mask:0xf\n\t"
                   "v_add_f32_dpp %4, %4, %4 row_ror:8 row_mask:0xf bank_mask:0xf\n\tv_add_f32_dpp %5, %5, %5 row_ror:8 row_mask:0xf bank_mask:0xf\n\t"
                   "v_add_f32_dpp %6, %6, %6 row_ror:8 row_mask:0xf bank_mask:0xf\n\tv_add_f32_dpp %7, %7, %7 row_ror:8 row_mask:0xf bank_mask:0xf\n\t"
                   "v_add_f32_dpp %0, %0, %0 row_ror:4 row_mask:0xf bank_mask:0xf\n\tv_add_f32_dpp %1, %1, %1 row_ror:4 row_mask:0xf bank_mask:0xf\n\t"
                   "v_add_f32_dpp %2, %2, %2 row_ror:4 row_mask:0xf bank_mask:0xf\n\tv_add_f32_dpp %3, %3, %3 row_ror:4 row_mask:0xf bank_mask:0xf\n\t"
                   "v_add_f32_dpp %4, %4, %4 row_ror:4 row_mask:0xf bank_mask:0xf\n\tv_add_f32_dpp %5, %5, %5 row_ror:4 row_mask:0xf bank_mask:0xf\n\t"
                   "v_add_f32_dpp %6, %6, %6 row_ror:4 row_mask:0xf bank_mask:0xf\n\tv_add_f32_dpp %7, %7, %7 row_ror:4 row_mask:0xf bank_mask:0xf"
                   : "+v"(acc[0][0]), "+v"(acc[0][1]), "+v"(acc[1][0]), "+v"(acc[1][1]), "+v"(acc[2][0]), "+v"(acc[2][1]), "+v"(acc[3][0]),
                     "+v"(acc[3][1]));
      if ((lane & 12) == 0) {
        LAS char* rp = L + L1_RED + (sw * 4 + q) * 128 + (lane & 3) * 32;
        *(LAS f32x4*)rp = f32x4{acc[0][0], acc[0][1], acc[1][0], acc[1][1]};
        *(LAS f32x4*)(rp + 16) = f32x4{acc[2][0], acc[2][1], acc[3][0], acc[3][1]};
      }
      if (sw < 2) {
        lds_arrive(L + L1_FLG + 4, lane);
      } else {
        lds_wait(L + L1_FLG + 4, 2);
        float pv[6];
#pragma unroll
        for (int i = 0; i < 6; ++i) pv[i] = *(const LAS float*)(L + L1_RED + ((lane >> 5) * 6 + i) * 128 + (lane & 31) * 4);
        float s = rbf(xor32f(((pv[0] + pv[1]) + (pv[2] + pv[3])) + (pv[4] + pv[5])));
        const int part = pidx / 18;
        if (part == 1 || part == 4) s = 1.f + s;
        const unsigned pr = pk2(s, dppf<0xb1>(0.f, s));
        if (lane < 32 && (lane & 1) == 0) {
          if (urgent.value)
            __hip_atomic_store((GAS unsigned long long*)(ws + WS_GRAN + pidx * 128 + lane * 4), ((unsigned long long)tag << 32) | pr,
                               RLX_AGENT);
          else *(unsigned*)(ws + WS_MOD + pidx * 64 + lane * 2) = pr;
        }
      }
      PRIO(0);
    };
    u32x4 fe[9], fo[9];
    if (tid >= 704 && tid < 706) *(LAS u32x4*)(L + L1_FLG + (tid & 1) * 16) = u32x4{0u, 0u, 0u, 0u};
    const int pu = (xcd >> 1) * 10 + jx, pd = (xcd >> 1) * 22 + jx - 10;
    if (g >= 2 && jx < 10 && pu < 36) {
      gemv(pu, std::true_type{});
    } else if (sw == 2) {
      const int uq = g < 2 ? (xcd >> 1) * 32 + jx : 128 + pd;
      const bool rt = g < 2 || (jx >= 10 && uq < 162);
      const int ks = uq / 9, nb = uq % 9;
      u32x4 rv[4];
      const __amdgpu_buffer_rsrc_t ro = __builtin_amdgcn_make_buffer_rsrc((void*)wq, 0, rt ? 576 * 1152 : 0, 0x00020000);
#pragma unroll
      for (int i = 0; i < 4; ++i)
        rv[i] = __builtin_amdgcn_raw_buffer_load_b128(ro, (lane >> 3) * 1152 + (lane & 7) * 16, (ks * 32 + i * 8) * 1152 + nb * 128, 0);
      SBAR();
      LOAD_TAG();
      SBAR();
      BAR_L();
      SBAR();
      MAKE_TAG();
      LAS char* R = L + L1_RT + 2 * 4608;
#pragma unroll
      for (int i = 0; i < 4; ++i) *(LAS u32x4*)(R + (i * 8 + (lane >> 3)) * 144 + (lane & 7) * 16) = rv[i];
      asm volatile("s_waitcnt lgkmcnt(0)" ::: "memory");
      const LAS char* tp = R + (8 * q + ((lane >> 2) & 3)) * 144 + (lane & 3) * 8;
      const __amdgpu_buffer_rsrc_t rd = __builtin_amdgcn_make_buffer_rsrc(ws + WS_WQ, 0, rt ? 663552 : 0, 0x00020000);
#pragma unroll
      for (int t = 0; t < 4; ++t) {
        const u32x2 lo = tr_read(tp + t * 32), hi = tr_read(tp + 4 * 144 + t * 32);
        __builtin_amdgcn_raw_buffer_store_b128(u32x4{lo[0], lo[1], hi[0], hi[1]}, rd, l16, ((nb * 4 + t) * 18 + ks) * 1024, AUX_SC1);
      }
      asm volatile("s_waitcnt vmcnt(0)" ::: "memory");
      if (lane == 0) __hip_atomic_store((GAS unsigned*)(ws + WS_ACK + b * 4), tag, RLX_AGENT);
    } else if (g >= 2) {
      BAR_L();
    } else {
      BAR_L();
      const __amdgpu_buffer_rsrc_t rw = __builtin_amdgcn_make_buffer_rsrc((void*)(g == 0 ? wk : wv), 0, 576 * 36, 0x00020000);
      const int vo = (sw * 32 + 8 * q) * 36 + n16 * 4;
#pragma unroll
      for (int s = 0; s < 9; ++s)
#pragma unroll
        for (int j = 0; j < 4; ++j) {
          const int r0 = 64 * s + 2 * j;
          const unsigned a0 = __builtin_amdgcn_raw_buffer_load_b32(rw, vo, r0 * 36, 0);
          const unsigned a1 = __builtin_amdgcn_raw_buffer_load_b32(rw, vo, (r0 + 1) * 36, 0);
          fe[s][j] = __builtin_amdgcn_perm(a1, a0, 0x05040100u);
          fo[s][j] = __builtin_amdgcn_perm(a1, a0, 0x07060302u);
        }
    }
    STAMP(1, 5);
    if (w == 11) {
      if (b == 0) { *(unsigned*)(ws + WS_CNT + lane * 128) = 0u; *(unsigned*)(ws + WS_CNT + 8192 + lane * 128) = 0u; }
      const __amdgpu_buffer_rsrc_t rg = __builtin_amdgcn_make_buffer_rsrc(ws + WS_ACK, 0, 1024 + 4608, 0x00020000);
      u32x4 gr[5];
      bool wdone = false;
      for (unsigned spins = 0; spins < SPIN_CAP; ++spins) {
        asm volatile("" ::: "memory");
        bool ok = true;
        const u32x4 ak = __builtin_amdgcn_raw_buffer_load_b128(rg, l16, 0, AUX_SC1);
#pragma unroll
        for (int i = 0; i < 5; ++i) gr[i] = __builtin_amdgcn_raw_buffer_load_b128(rg, l16, 1024 + i * 1024, AUX_SC1);
        if (!wdone && __all(ak[0] == tag && ak[1] == tag && ak[2] == tag && ak[3] == tag)) {
          wdone = true;
          lds_arrive(L + L1_FLG, lane);
        }
#pragma unroll
        for (int i = 0; i < 5; ++i) ok = ok && ((gr[i][1] == tag && gr[i][3] == tag) || (i == 4 && lane >= 32));
        if (wdone && __all(ok)) break;
      }
#pragma unroll
      for (int i = 0; i < 5; ++i)
        if (i < 4 || lane < 32)
          *(LAS f32x4*)(L + L1_M + i * 1024 + l16) = f32x4{bflo(gr[i][0]), bfhi(gr[i][0]), bflo(gr[i][2]), bfhi(gr[i][2])};
      lds_arrive(L + L1_FLG + 16, lane);
      if (b == 0 && lane == 0) __hip_atomic_store((GAS unsigned*)(ws + WS_EPOCH), tag, RLX_AGENT);
    } else if (g >= 2) {
      lds_wait(L + L1_FLG, 1);
    }
    STAMP(1, 7);
    if (g < 2 && sw < 2) {
      lds_wait(L + L1_FLG + 8, 9);
      STAMP(1, 8);
      f32x4 ae[2] = {}, ao[2] = {};
      u32x4 hf[18];
#pragma unroll
      for (int i = 0; i < 18; ++i) hf[i] = *(const LAS u32x4*)((sw ? hb1 : hb0) + (i & 1) * 18432 + (2 * (i >> 1) + sw) * 1024);
      SBAR();
#pragma unroll
      for (int i = 0; i < 18; ++i) {
        ae[i & 1] = mfma16(fe[i >> 1], hf[i], ae[i & 1]);
        ao[i & 1] = mfma16(fo[i >> 1], hf[i], ao[i & 1]);
      }
#pragma unroll
      for (int mt = 0; mt < 2; ++mt) {
        LAS char* op = L + L1_OUT + (sw * 32 + mt * 16 + n16) * 784 + 640 + q * 32;
        *(LAS f32x4*)op = f32x4{ae[mt][0], ao[mt][0], ae[mt][1], ao[mt][1]};
        *(LAS f32x4*)(op + 16) = f32x4{ae[mt][2], ao[mt][2], ae[mt][3], ao[mt][3]};
      }
      lds_arrive(L + L1_FLG + 12, lane);
    } else if (g >= 2) {
      const int uu = ((xcd >> 1) * 32 + jx) + 128 * sw;
      const bool rt = sw < 2 && uu < 162;
      const int rs_ = uu / 9, nb = uu % 9, st = rs_ >> 1, kk = rs_ & 1;
      u32x4 rv[4];
      {
        const int k0 = st < 8 ? 72 * st + 32 * kk : 288 * kk + 64, kq = st < 8 ? 8 : 72;
        const __amdgpu_buffer_rsrc_t ro = __builtin_amdgcn_make_buffer_rsrc((void*)wo, 0, rt ? 576 * 1152 : 0, 0x00020000);
#pragma unroll
        for (int i = 0; i < 4; ++i)
          rv[i] = __builtin_amdgcn_raw_buffer_load_b128(ro, (lane >> 3) * 1152 + (lane & 7) * 16, (k0 + i * kq) * 1152 + nb * 128, 0);
      }
      SBAR();
      if (jx >= 10 && pd < 72) gemv(36 + pd, std::false_type{});
      PIN(rv[0]); PIN(rv[1]); PIN(rv[2]); PIN(rv[3]);
      if (rt) {
        LAS char* R = L + L1_RT + sw * 4608;
#pragma unroll
        for (int i = 0; i < 4; ++i) *(LAS u32x4*)(R + (i * 8 + (lane >> 3)) * 144 + (lane & 7) * 16) = rv[i];
        asm volatile("s_waitcnt lgkmcnt(0)" ::: "memory");
        const LAS char* tp = R + (8 * q + ((lane >> 2) & 3)) * 144 + (lane & 3) * 8;
#pragma unroll
        for (int t = 0; t < 4; ++t) {
          const int tt = nb * 4 + t, p = tt / 9, i = tt % 9;
          const int wv_ = i < 4 ? p : (i > 4 ? p + 4 : p + 4 * kk), un = i == 4 ? 8 : (i < 4 ? kk * 4 + i : (kk ^ 1) * 4 + i - 5);
          const u32x2 lo = tr_read(tp + t * 32), hi = tr_read(tp + 4 * 144 + t * 32);
          *(u32x4*)(ws + WS_WO + (size_t)((st * 8 + wv_) * 9 + un) * 1024 + lane * 16) = u32x4{lo[0], lo[1], hi[0], hi[1]};
        }
      }
      STAMP(1, 9);
      STAMP_FLUSH(1);
      return;
    }
  }
  if (w < 10) lds_wait(L + L1_FLG + 12, g < 2 ? 11 : 9);
  STAMP(1, 9);
  const int r = lane & 31, h = lane >> 5;
  if (w < 8 || (w == 8 && g == 0)) {
    const bool isq = w < 8;
    const int tok = isq ? (w >> 1) * 8 + (r & 7) : r, col = isq ? ((w & 1) * 4 + (r >> 3)) * 18 : 160;
    const LAS char* ip = L + L1_OUT + tok * 784 + col * 4;
    f32x2 v[5];
#pragma unroll
    for (int j = 0; j < 4; ++j) v[j] = *(const LAS f32x2*)(ip + h * 32 + j * 8);
    v[4] = *(const LAS f32x2*)(ip + 64);
    if (!isq) {
#pragma unroll
      for (int j = 0; j < 4; ++j) v[j] += *(const LAS f32x2*)(ip + 25088 + h * 32 + j * 8);
      v[4] += *(const LAS f32x2*)(ip + 25088 + 64);
    }
    const float sc = isq ? QSCALE : 1.f;
    const u32x4 o = {pk2(v[0][0] * sc, v[0][1] * sc), pk2(v[1][0] * sc, v[1][1] * sc), pk2(v[2][0] * sc, v[2][1] * sc),
                     pk2(v[3][0] * sc, v[3][1] * sc)};
    const unsigned o2 = h == 0 ? pk2(v[4][0] * sc, v[4][1] * sc) : 0u;
    float n2 = bflo(o2) * bflo(o2) + bfhi(o2) * bfhi(o2);
#pragma unroll
    for (int j = 0; j < 4; ++j) n2 = fmaf(bflo(o[j]), bflo(o[j]), fmaf(bfhi(o[j]), bfhi(o[j]), n2));
    n2 = xor32f(n2);
    if (isq) {
      const size_t fi = (size_t)(T * 4 + (w >> 1)) * 8 + g * 2 + (w & 1);
      *(u32x4*)(ws + WS_QF + fi * 1024 + lane * 16) = o;
      const unsigned nb = (__float_as_uint(__builtin_amdgcn_sqrtf(n2) * 1.0001f) + 0xffffu) >> 16;
      *(unsigned*)(ws + WS_QF2 + fi * 256 + lane * 4) = h == 0 ? o2 : nb;
    } else {
      *(u32x4*)(ws + WS_KF + (size_t)T * 1024 + lane * 16) = o;
      if (h == 0) *(unsigned*)(ws + WS_KF2 + (((T >> 3) * 32 + r) * 8 + (T & 7)) * 4) = o2;
      n2 = wave_max_nonneg(n2);
      if (lane == 0) *(float*)(ws + WS_KMAX + T * 4) = n2;
    }
  } else if (w < 10 && g == 1) {
    const int s = w - 8;
    const LAS char* ip = L + L1_OUT + (16 * s + 4 * h) * 784 + (160 + r) * 4;
    float v[8];
#pragma unroll
    for (int j = 0; j < 8; ++j) {
      const int to = (8 * (j >> 2) + (j & 3)) * 784;
      v[j] = *(const LAS float*)(ip + to) + *(const LAS float*)(ip + 25088 + to);
    }
    u32x4 o = {pk2(v[0], v[1]), pk2(v[2], v[3]), pk2(v[4], v[5]), pk2(v[6], v[7])};
    if (r >= 18) { const unsigned cc = r == 18 ? 0x3f803f80u : 0u; o = u32x4{cc, cc, cc, cc}; }
    if (r < 20) *(u32x4*)(ws + WS_VF + (size_t)(((T * 2 + s) * 2 + h) * 20 + r) * 16) = o;
  }
  STAMP_FLUSH(1);
}

constexpr int L2_WO = 0, L2_Q = 73728, L2_RT = 81920, L2_LO = L2_RT + 16384, L2_PB = L2_LO + 16384, L2_OB = L2_PB + 22272,
              L2_Q2 = L2_OB + 9216, L2_X = L2_Q2 + 2048, L2_M = L2_X + 9216, L2_FLAG = L2_M + 4096, L2_BYTES = L2_FLAG + 16;
static_assert(L2_BYTES <= 163840, "LDS");
#define XFB

DI int ob_off(int g, int cl, int tok) {
  return cl < 64 ? g * 1024 + (cl >> 3) * 128 + tok * 16 + (cl & 7) * 2 : 8192 + g * 128 + tok * 16 + (cl - 64) * 2;
}

__global__ __launch_bounds__(512, 2) void k2_attn(const u16* __restrict__ x, const u16* __restrict__ w1, const u16* __restrict__ w2,
                                                  char* __restrict__ ws) {
  extern __shared__ __attribute__((aligned(16))) char smem[];
  LAS char* L = (LAS char*)smem;
  const unsigned lds0 = (unsigned)(uintptr_t)L;
  const int tid = threadIdx.x, lane = tid & 63, w = __builtin_amdgcn_readfirstlane(tid >> 6), b = blockIdx.x;
  const int r = lane & 31, h = lane >> 5;
  const unsigned l16 = lane * 16;
  STAMP_INIT();
  STAMP(2, 0);
  glds16s<0>(ws + WS_QF + (size_t)(b * 8 + w) * 1024, l16, lds0 + L2_Q + w * 1024);
  glds4(ws + WS_QF2 + ((size_t)(b * 8 + w) * 64 + lane) * 4, lds0 + L2_Q2 + w * 256);
  if (tid == 0) *(LAS unsigned*)(L + L2_FLAG) = 0u;
  const float km_in = *(const float*)(ws + WS_KMAX + lane * 4);
  bf16x8 Q1 = *(const bf16x8*)(ws + WS_QF + (size_t)b * 8192 + l16);
  const unsigned q2 = *(const unsigned*)(ws + WS_QF2 + (size_t)b * 2048 + lane * 4);
  bf16x8 Kf[8], Vf[16];
  u32x4 K2a = *(const u32x4*)(ws + WS_KF2 + (w * 32 + r) * 32), K2b = *(const u32x4*)(ws + WS_KF2 + (w * 32 + r) * 32 + 16);
  const char* kp = ws + WS_KF + (size_t)w * 8192 + l16;
  const char* vp = ws + WS_VF + (size_t)w * 10240 + (h * 20 + (r < 19 ? r : 19)) * 16;
#define LK(t) { Kf[t] = *(const bf16x8*)(kp + (t) * 1024); }
#define LV(i) { Vf[i] = *(const bf16x8*)(vp + (i) * 640); }
  SBAR();
  LK(0); LK(1); SBAR(); LV(0); LK(2); SBAR(); LV(1); LV(2); SBAR();
  const int rph = b & 1, rld = rph ? 1152 : 9216;
  unsigned rvo;
  {
    const int rr = (w & 3) * 8 + (lane >> 3);
    rvo = ((w >> 2) * 32 + (rph ? rr : 8 * ((rr >> 2) & 3) + 4 * (rr >> 4) + (rr & 3))) * rld + (lane & 7) * 16;
  }
  unsigned rra = lds0 + L2_RT + ((w >> 2) * 32 + (lane >> 4) * 4 + ((lane >> 2) & 3)) * 128 + (w & 3) * 32 + (lane & 3) * 8;
  const __amdgpu_buffer_rsrc_t rwf = __builtin_amdgcn_make_buffer_rsrc(ws + WS_WF, 0, 10616832 + 0, 0x00020000);
  const __amdgpu_buffer_rsrc_t rpt = __builtin_amdgcn_make_buffer_rsrc(ws + WS_PART + (size_t)b * 73728 + w * 9216, 0, 9216, 0x00020000);
  int rsrcv, rdstv;
  {
    const int b0 = (b >> 1) + 128 * (lane & 7), blk = b0 < 648 ? b0 : (b >> 1) + 512;
    const int kb = rph ? blk / 9 : blk / 72, nb = rph ? blk % 9 : blk % 72;
    const int ks = kb * 2 + (w >> 2), tt = nb * 4 + (w & 3);
    rsrcv = kb * 64 * rld + nb * 128;
    rdstv = (rph ? wf_index(1, ks / 18, ks % 18, tt) : wf_index(0, tt / 36, ks, tt % 36)) * 1024;
    PIN(rsrcv); PIN(rdstv);
  }
  const char* rbase = (const char*)(rph ? w2 : w1);
  u32x2 rlo, rhi;
#define rt_load(i) glds16s<0>(rbase + __builtin_amdgcn_readlane(rsrcv, i), rvo, lds0 + L2_RT + ((i) & 1) * 8192 + w * 1024)
#define rt_store(i) __builtin_amdgcn_raw_buffer_store_b128(u32x4{rlo[0], rlo[1], rhi[0], rhi[1]}, rwf, l16, __builtin_amdgcn_readlane(rdstv, i), AUX_SC1)
  SBAR();
  {
    const float km = wave_max_nonneg(km_in);
    const unsigned nk = ((__float_as_uint(__builtin_amdgcn_sqrtf(km) * 1.0001f) + 0xffffu) >> 16) | 0x8000u;
#pragma unroll
    for (int t = 0; t < 4; ++t) { K2a[t] = h == 0 ? K2a[t] : nk; K2b[t] = h == 0 ? K2b[t] : nk; }
  }
  STAMP(2, 1);
  f32x4 oacc[5];
#pragma unroll
  for (int i = 0; i < 5; ++i) oacc[i] = f32x4{0.f, 0.f, 0.f, 0.f};
  const f32x16 zero16 = {};
  f32x16 O, Oold, SA, SB;
  u32x2 qa = {q2, 0u}, qb = {0u, q2};
  u32x4 Plo, Phi = {0u, 0u, 0u, 0u};
  float e14 = 0.f, e15 = 0.f;
#define KPAIR(t) __builtin_bit_cast(s16x4, (t) < 4 ? u32x2{K2a[(t) & 2], K2a[((t) & 2) + 1]} : u32x2{K2b[(t) & 2], K2b[((t) & 2) + 1]})
#define QPAIR(t) __builtin_bit_cast(s16x4, ((t) & 1) ? qb : qa)
#define BF8(x) __builtin_bit_cast(bf16x8, x)
  SA = mfma32k8(KPAIR(0), QPAIR(0), zero16);
  SA = mfma32(Kf[0], Q1, SA);
#define EEC(cur, i, P, j)                                                                               \
  {                                                                                                     \
    float a_ = EX2(cur[i]), b_ = EX2(cur[(i) + 1]);                                                     \
    PIN(a_); PIN(b_); SBAR();                                                                           \
    P[j] = pk2(e14, e15); PIN(P[j]); SBAR();                                                            \
    e14 = a_; e15 = b_;                                                                                 \
  }
#define TILE(t, cur, nxt, M0, S0, M1, S1, M2, S2, M3, S3)                                               \
  {                                                                                                     \
    nxt = mfma32k8(KPAIR(((t) + 1) & 7), QPAIR(((t) + 1) & 7), zero16); SBAR();                         \
    EEC(cur, 0, Phi, 3); M0; SBAR(); EEC(cur, 2, Plo, 0); S0; SBAR();                                   \
    nxt = mfma32(Kf[((t) + 1) & 7], Q1, nxt); SBAR();                                                   \
    EEC(cur, 4, Plo, 1); M1; SBAR(); EEC(cur, 6, Plo, 2); S1; SBAR();                                   \
    if ((t) != 0) O = mfma32(Vf[(2 * (t) + 15) & 15], BF8(Phi), O);                                     \
    else if (!FIRST) asm volatile("v_mfma_f32_32x32x16_bf16 %0, %1, %2, %3" : "=&v"(Oold) : "v"(Vf[15]), "v"(Phi), "v"(O)); \
    SBAR();                                                                                             \
    EEC(cur, 8, Plo, 3); M2; SBAR(); EEC(cur, 10, Phi, 0); S2; SBAR();                                  \
    if ((t) == 0 && !FIRST) asm volatile("" ::"v"(O));                                                  \
    O = mfma32(Vf[2 * (t)], BF8(Plo), (t) == 0 ? zero16 : O);                                           \
    SBAR();                                                                                             \
    EEC(cur, 12, Phi, 1); M3; SBAR(); EEC(cur, 14, Phi, 2); S3; SBAR();                                 \
  }
  LAS char* pbw = L + L2_PB + w * 2720 + r * 16 + h * 544;
  LAS char* pbw2 = L + L2_PB + (h ? 21760 : w * 2720 + 2176) + r * 16;
#define WP0() { PIN(Oold); *(LAS f32x4*)(pbw) = f32x4{Oold[0], Oold[1], Oold[2], Oold[3]}; }
#define WP1() { *(LAS f32x4*)(pbw + 1088) = f32x4{Oold[4], Oold[5], Oold[6], Oold[7]}; }
#define WP2() { *(LAS f32x4*)(pbw2) = f32x4{Oold[8], Oold[9], Oold[10], Oold[11]}; }
  const int rrow = 4 * w + (lane >> 4), rp = (lane & 15) < 10 ? (lane & 15) : 9;
  const LAS char* rsrc = L + L2_PB + (rp >> 1) * 544 + (4 * w + ((lane >> 4) ^ 0)) * 16 + (rp & 1) * 8;
  const int rcl = 18 * (rrow >> 3) + 2 * rp;
  unsigned odst = lds0 + ((lane & 15) < 9 ? L2_OB + ob_off(0, rcl, rrow & 7) : L2_FLAG + 8);
  const int ostr = (lane & 15) < 9 ? (rcl < 64 ? 1024 : 128) : 0;
  unsigned long long bad = 0ull;
  f32x2 pv[8];
  float il_;
#define RP(i) { pv[i] = *(const LAS f32x2*)(rsrc + (i) * 2720); pv[(i) + 1] = *(const LAS f32x2*)(rsrc + ((i) + 1) * 2720); }
#define ADD2(a, b) { pv[a] += pv[b]; PIN(pv[a]); }
#define RB0() { const float l_ = __builtin_bit_cast(float, __builtin_amdgcn_mov_dpp(__builtin_bit_cast(int, pv[0][0]), 0x159, 0xf, 0xf, true)); \
                bad |= __builtin_amdgcn_fcmpf(l_, 0x1p-64f, 12  ); il_ = __builtin_amdgcn_rcpf(l_); PIN(il_); }
#define LP(T, a) ((LAS T*)(uintptr_t)(a))
#define RB1() { *LP(unsigned, odst) = pk2(pv[0][0] * il_, pv[0][1] * il_); odst += ostr; PIN(odst); }
  u32x4 oA, oB, wu[3];
  const LAS char* ord = L + L2_OB + (lane >> 4) * 128 + (lane & 7) * 16;
  const LAS char* wrd = L + L2_WO + w * 9216 + l16;
  const int hi512 = (w >> 2) * 512;
#define RO(st) { oA = *(const LAS u32x4*)(ord + (st) * 1024 + hi512); oB = *(const LAS u32x4*)(ord + (st) * 1024 + (hi512 ^ 512)); }
#define RU(u) { wu[(u) % 3] = *(const LAS u32x4*)(wrd + (u) * 1024); }
#define MU(u) { oacc[(u) < 8 ? (u) & 3 : 4] = mfma16(wu[(u) % 3], ((u) < 4 || (u) == 8) ? oA : oB, oacc[(u) < 8 ? (u) & 3 : 4]); }
  const char* wsrc = ws + WS_WO + (size_t)w * 9216;
#define GW(st, u) glds16s<((u) & 3) * 1024>(wsrc + (size_t)(st) * 73728 + ((u) >> 2) * 4096, l16, lds0 + L2_WO + w * 9216 + ((u) >> 2) * 4096)
  const LAS char* qrd = L + L2_Q + l16;
  const LAS char* q2rd = L + L2_Q2 + lane * 4;
#define RQ1(gn) { Q1 = *(const LAS bf16x8*)(qrd + (gn) * 1024); }
#define RQA(gn) { qa[0] = *(const LAS unsigned*)(q2rd + (gn) * 256); }
#define RQB(gn) { qb[1] = *(const LAS unsigned*)(q2rd + (gn) * 256); }
#define NOP {}
#define GL(u) glds16s<(u) * 1024>(wsrc + (size_t)8 * 73728, l16, lds0 + L2_LO + w * 2048)
#define RTR() { rlo = tr_read(LP(const char, rra)); rhi = tr_read(LP(const char, rra + 2048)); rra ^= 8192; PIN(rra); }
#define PT(i) __builtin_amdgcn_raw_buffer_store_b128(Phi, rpt, l16, (i) * 1024, AUX_SC1)
  {
    constexpr bool FIRST = true;
    PRIO(3);
    TILE(0, SA, SB, NOP, LK(3), NOP, LV(3), NOP, LV(4), NOP, LK(4));
    TILE(1, SB, SA, NOP, LV(5), NOP, LV(6), NOP, LK(5), NOP, { LV(7); PRIO(2); });
    TILE(2, SA, SB, NOP, LV(8), NOP, LK(6), NOP, LV(9), NOP, { LV(10); PRIO(1); });
    TILE(3, SB, SA, NOP, LK(7), NOP, LV(11), NOP, LV(12), NOP, { LV(13); PRIO(0); });
    TILE(4, SA, SB, NOP, LV(14), NOP, LV(15), NOP, NOP, NOP, { BAR_L(); PRIO(3); });
    TILE(5, SB, SA, NOP, RQA(1), NOP, NOP, NOP, NOP, NOP, PRIO(2));
    TILE(6, SA, SB, RQB(1), { PIN(Vf[12]); PIN(Vf[13]); PIN(Vf[14]); PIN(Vf[15]); }, { RQ1(1); GW(0, 0); }, GW(0, 1), GW(0, 2), GW(0, 3),
         GW(0, 4), { GW(0, 5); PRIO(1); });
    TILE(7, SB, SA, GW(0, 6), GW(0, 7), GW(0, 8), GL(0), GL(1), rt_load(0), PT(0), PT(8));
    glds16s<0>((const char*)x + (size_t)b * 9216 + w * 1024, l16, lds0 + L2_X + w * 1024);
    if (w == 0) glds16s<0>((const char*)x + (size_t)b * 9216 + 8192, l16, lds0 + L2_X + 8192);
    else if (w < 5) glds16s<0>(ws + WS_MOD + 2 * D_ * 2 + (w - 1) * 1024, l16, lds0 + L2_M + (w - 1) * 1024);
    STAMP(2, 2);
  }
  unsigned ora = lds0 + L2_OB + (lane >> 4) * 128 + (lane & 7) * 16 + hi512, orb = ora + 512 - 2 * hi512, qra = lds0 + L2_Q + l16 + 2048,
           q2a = lds0 + L2_Q2 + lane * 4 + 512;
  PIN(ora); PIN(orb); PIN(qra); PIN(q2a);
#define ROL() { oA = *LP(const u32x4, ora); oB = *LP(const u32x4, orb); }
  for (int g = 1; g < 8; ++g) {
    constexpr bool FIRST = false;
    TILE(0, SA, SB, NOP, NOP, NOP, NOP, NOP, NOP, NOP, PRIO(0));
    TILE(1, SB, SA, WP0(), WP1(), WP2(), NOP, NOP, NOP, NOP, { BAR_L(); PRIO(3); });
    TILE(2, SA, SB, RP(0), RP(2), RP(4), { RP(6); PRIO(2); }, NOP, { ADD2(0, 1); ADD2(2, 3); }, NOP, { ADD2(4, 5); ADD2(6, 7); PRIO(1); });
    TILE(3, SB, SA, NOP, { ADD2(0, 2); ADD2(4, 6); }, NOP, { ADD2(0, 4); PRIO(0); }, RB0(), RB1(), NOP,
         { asm volatile("s_waitcnt vmcnt(0)" ::: "memory"); BAR_L(); PRIO(3); });
    TILE(4, SA, SB, ROL(), RU(0), rt_load(g), RU(1), MU(0), RU(2), MU(1), { RU(3); GW(g, 0); });
    TILE(5, SB, SA, MU(2), { RU(4); GW(g, 1); qa[0] = *LP(const unsigned, q2a); }, MU(3), { RU(5); PRIO(2); }, MU(4), { RU(6); GW(g, 2); }, MU(5),
         { RU(7); GW(g, 3); });
    TILE(6, SA, SB, MU(6), { RU(8); qb[1] = *LP(const unsigned, q2a); }, MU(7), { Q1 = *LP(const bf16x8, qra); GW(g, 4); }, MU(8), GW(g, 5), RTR(),
         { ora += 1024; orb += 1024; PIN(ora); PIN(orb); });
    TILE(7, SB, SA, rt_store(g - 1), { GW(g, 6); PRIO(1); }, PT(g), GW(g, 7), NOP, { qra += 1024; q2a += 256; PIN(qra); PIN(q2a); }, NOP, GW(g, 8));
  }
  STAMP(2, 3);
  PRIO(0);
  u32x4 wl[8];
  wl[6] = *(const u32x4*)(wsrc + (size_t)8 * 73728 + 2048 + l16);
  wl[7] = *(const u32x4*)(wsrc + (size_t)8 * 73728 + 3072 + l16);
#pragma unroll
  for (int i = 0; i < 5; ++i) wl[i] = *(const u32x4*)(wsrc + (size_t)8 * 73728 + (4 + i) * 1024 + l16);
  wl[5] = *(const u32x4*)(ws + WS_WO + (size_t)8 * 73728 + ((w | 4) * 9 + 8) * 1024 + l16);
  SBAR();
  Phi[3] = pk2(e14, e15);
  Oold = mfma32(Vf[15], BF8(Phi), O);
  LAS char* xb = L + L2_Q + (w & 3) * 1024 + l16;
  if (w >= 4) *(LAS f32x4*)xb = oacc[4];
  WP0(); WP1(); WP2();
  BAR_L();
  RP(0); RP(2); RP(4); RP(6);
  oacc[4] += *(const LAS f32x4*)xb;
  ADD2(0, 1); ADD2(2, 3); ADD2(4, 5); ADD2(6, 7); ADD2(0, 2); ADD2(4, 6); ADD2(0, 4); RB0(); RB1();
  if (bad != 0ull) *(LAS unsigned*)(L + L2_FLAG) = 1u;
  asm volatile("s_waitcnt vmcnt(8)" ::: "memory");
  BAR_L();
  if (*(volatile LAS unsigned*)(L + L2_FLAG) != 0u XFB) {
    const int rw = tid >> 1, hf = tid & 1;
    const int gb = rw >> 5, l32 = rw & 31;
    float qv[18];
    {
      const u16* qa = (const u16*)(ws + WS_QF + (size_t)(b * 8 + gb) * 1024);
      for (int j = 0; j < 8; ++j) {
        qv[(j & 3) + 8 * (j >> 2)] = bf2f(qa[l32 * 8 + j]);
        qv[(j & 3) + 8 * (j >> 2) + 4] = bf2f(qa[(32 + l32) * 8 + j]);
      }
      const unsigned q2v = *(const LAS unsigned*)(L + L2_Q2 + gb * 256 + l32 * 4);
      qv[16] = bflo(q2v); qv[17] = bfhi(q2v);
    }
    float mx = -INFINITY, l = 0.f, ov[18];
#pragma unroll
    for (int d = 0; d < 18; ++d) ov[d] = 0.f;
    for (int pass = 0; pass < 2; ++pass) {
      for (int key = hf * 1024; key < hf * 1024 + 1024; ++key) {
        const int T = key >> 5, kr = key & 31;
        const u32x4 ka_ = *(const u32x4*)(ws + WS_KF + ((size_t)T * 64 + kr) * 16);
        const u32x4 kb_ = *(const u32x4*)(ws + WS_KF + ((size_t)T * 64 + 32 + kr) * 16);
        const unsigned kc_ = *(const unsigned*)(ws + WS_KF2 + (((T >> 3) * 32 + kr) * 8 + (T & 7)) * 4);
        float s = qv[16] * bflo(kc_) + qv[17] * bfhi(kc_);
#pragma unroll
        for (int j = 0; j < 4; ++j) {
          const int d0 = (2 * j & 3) + 8 * (j >> 1);
          s += qv[d0] * bflo(ka_[j]) + qv[d0 + 1] * bfhi(ka_[j]) + qv[d0 + 4] * bflo(kb_[j]) + qv[d0 + 5] * bfhi(kb_[j]);
        }
        if (pass == 0) mx = fmaxf(mx, s);
        else {
          const float p = rbf(EX2(s - mx));
          l += p;
          const int st = kr >> 4, hh = (kr >> 2) & 1, j = ((kr >> 3) & 1) * 4 + (kr & 3);
          const u16* vp = (const u16*)(ws + WS_VF + (size_t)(((T * 2 + st) * 2 + hh) * 20) * 16) + j;
#pragma unroll
          for (int d = 0; d < 18; ++d) ov[d] += p * bf2f(vp[d * 8]);
        }
      }
      if (pass == 0) mx = fmaxf(mx, __shfl_xor(mx, 1));
    }
    l += __shfl_xor(l, 1);
    const float il = 1.f / l;
#pragma unroll
    for (int d = 0; d < 18; ++d) ov[d] = (ov[d] + __shfl_xor(ov[d], 1)) * il;
    if (hf == 0) {
#pragma unroll
      for (int d = 0; d < 18; d += 2) *(LAS unsigned*)(L + L2_OB + ob_off(gb, 18 * (l32 >> 3) + d, l32 & 7)) = pk2(ov[d], ov[d + 1]);
    }
    __syncthreads();
#pragma unroll
    for (int i = 0; i < 5; ++i) oacc[i] = f32x4{0.f, 0.f, 0.f, 0.f};
    for (int st = 0; st < 7; ++st) {
      RO(st);
      const char* wp_ = wsrc + (size_t)st * 73728 + l16;
#pragma unroll
      for (int u = 0; u < 8; ++u) oacc[u & 3] = mfma16(*(const u32x4*)(wp_ + u * 1024), u < 4 ? oA : oB, oacc[u & 3]);
      const char* ws_ = ws + WS_WO + (size_t)st * 73728 + (w & 3) * 9216 + 8192 + l16;
      oacc[4] = mfma16(*(const u32x4*)ws_, oA, oacc[4]);
      oacc[4] = mfma16(*(const u32x4*)(ws_ + 4 * 9216), oB, oacc[4]);
    }
  }
  STAMP(2, 4);
  {
    u32x4 wt[14], oC, oD;
    RO(7);
    oC = *(const LAS u32x4*)(ord + 8192 + hi512); oD = *(const LAS u32x4*)(ord + 8192 + (hi512 ^ 512));
#pragma unroll
    for (int u = 0; u < 9; ++u) wt[u] = *(const LAS u32x4*)(wrd + u * 1024);
    wt[13] = *(const LAS u32x4*)(L + L2_WO + (w | 4) * 9216 + 8192 + l16);
#pragma unroll
    for (int u = 0; u < 2; ++u) wt[9 + u] = *(const LAS u32x4*)(L + L2_LO + w * 2048 + u * 1024 + l16);
    SBAR();
#pragma unroll
    for (int u = 0; u < 8; ++u) oacc[u & 3] = mfma16(wt[u], u < 4 ? oA : oB, oacc[u & 3]);
    oacc[4] = mfma16(wt[8], oA, oacc[4]);
    oacc[4] = mfma16(wt[13], oB, oacc[4]);
#pragma unroll
    for (int u = 0; u < 4; ++u) oacc[u] = mfma16(u < 2 ? wt[9 + u] : wl[4 + u], oC, oacc[u]);
    oacc[4] = mfma16(wl[4], oC, oacc[4]);
#pragma unroll
    for (int u = 0; u < 4; ++u) oacc[u] = mfma16(wl[u], oD, oacc[u]);
    oacc[4] = mfma16(wl[5], oD, oacc[4]);
  }
  if ((lane & 8) == 0) {
    LAS char* eb = L + L2_PB + (lane & 7) * 2320 + (lane >> 4) * 16 + (9 * (w & 3) + (w >> 2) * 5) * 64;
#pragma unroll
    for (int i = 0; i < 4; ++i) *(LAS f32x4*)(eb + i * 64) = oacc[i];
    if (w < 4) *(LAS f32x4*)(eb + 256) = oacc[4];
  }
  BAR_L();
  {
    const LAS char* er = L + L2_PB + w * 2320;
    const f32x4 a0 = *(const LAS f32x4*)(er + lane * 32), a1 = *(const LAS f32x4*)(er + lane * 32 + 16);
    const float a8 = *(const LAS float*)(er + 2048 + lane * 4);
    const u32x4 xv = *(const LAS u32x4*)(L + L2_X + w * 1152 + l16), gv = *(const LAS u32x4*)(L + L2_M + l16);
    const unsigned x8 = *(const LAS u16*)(L + L2_X + w * 1152 + 1024 + lane * 2), g8 = *(const LAS u16*)(L + L2_M + 1024 + lane * 2);
    const u32x4 sv = *(const LAS u32x4*)(L + L2_M + 1152 + l16), cv = *(const LAS u32x4*)(L + L2_M + 2304 + l16);
    const unsigned s8 = *(const LAS u16*)(L + L2_M + 1152 + 1024 + lane * 2), c8 = *(const LAS u16*)(L + L2_M + 2304 + 1024 + lane * 2);
    u32x4 xp;
    xp[0] = pk2(fmaf(bflo(gv[0]), a0[0], bflo(xv[0])), fmaf(bfhi(gv[0]), a0[1], bfhi(xv[0])));
    xp[1] = pk2(fmaf(bflo(gv[1]), a0[2], bflo(xv[1])), fmaf(bfhi(gv[1]), a0[3], bfhi(xv[1])));
    xp[2] = pk2(fmaf(bflo(gv[2]), a1[0], bflo(xv[2])), fmaf(bfhi(gv[2]), a1[1], bfhi(xv[2])));
    xp[3] = pk2(fmaf(bflo(gv[3]), a1[2], bflo(xv[3])), fmaf(bfhi(gv[3]), a1[3], bfhi(xv[3])));
    const unsigned xp8 = pk2(fmaf(bflo(g8), a8, bflo(x8)), 0.f);
    char* x1p = ws + WS_X1 + (size_t)(b * 8 + w) * 1152;
    *(u32x4*)(x1p + l16) = xp;
    *(u16*)(x1p + 1024 + lane * 2) = (u16)xp8;
    float v[9];
#pragma unroll
    for (int j = 0; j < 4; ++j) { v[2 * j] = bflo(xp[j]); v[2 * j + 1] = bfhi(xp[j]); }
    v[8] = bflo(xp8);
    const float mean = wave_sum(((v[0] + v[1]) + (v[2] + v[3])) + ((v[4] + v[5]) + (v[6] + v[7])) + v[8]) * (1.f / D_);
    float m2 = 0.f;
#pragma unroll
    for (int j = 0; j < 9; ++j) { v[j] -= mean; m2 = fmaf(v[j], v[j], m2); }
    const float rstd = __builtin_amdgcn_rsqf(wave_sum(m2) * (1.f / D_) + LN_EPS);
    u32x4 hp;
#pragma unroll
    for (int j = 0; j < 4; ++j)
      hp[j] = pk2(fmaf(v[2 * j] * rstd, bflo(cv[j]), bflo(sv[j])), fmaf(v[2 * j + 1] * rstd, bfhi(cv[j]), bfhi(sv[j])));
    const unsigned hp8 = pk2(fmaf(v[8] * rstd, bflo(c8), bflo(s8)), 0.f);
    *(LAS u32x4*)(L + L2_OB + lane * 128 + ((w ^ lane) & 7) * 16) = hp;
    *(LAS u16*)(L + L2_OB + (64 + (lane >> 3)) * 128 + ((w ^ (lane >> 3)) & 7) * 16 + (lane & 7) * 2) = (u16)hp8;
  }
  BAR_L();
  {
    char* hb = ws + WS_H2 + (size_t)(b >> 1) * 18432 + (b & 1) * 128 + (tid >> 3) * 256 + (tid & 7) * 16;
    const LAS char* hs = L + L2_OB + (tid >> 3) * 128 + ((tid ^ (tid >> 3)) & 7) * 16;
    *(u32x4*)hb = *(const LAS u32x4*)hs;
    if (w == 0) *(u32x4*)(hb + 64 * 256) = *(const LAS u32x4*)(hs + 8192);
  }
  STAMP(2, 5);
  STAMP_FLUSH(2);
}

constexpr int L3_A = 0, L3_H = 73728, L3_FLG = 147456, L3_BYTES = L3_FLG + 16;
#define K3_WIN 12
typedef short s16x2 __attribute__((ext_vector_type(2)));
DI unsigned relu2(unsigned v) {
  return __builtin_bit_cast(unsigned, __builtin_elementwise_max(__builtin_bit_cast(s16x2, v), s16x2{0, 0}));
}
DI void poll_ge(char* p, unsigned n) {
  for (unsigned spins = 0; spins < SPIN_CAP; ++spins) {
    if (__builtin_amdgcn_readfirstlane(__hip_atomic_load((GAS unsigned*)p, RLX_AGENT)) >= n) break;
    __builtin_amdgcn_s_sleep(2);
  }
  __builtin_amdgcn_fence(__ATOMIC_ACQUIRE, "wavefront");
}
DI void wg_signal(LAS char* lp, char* gp, int lane) {
  if (lane == 0) {
    const unsigned old = __hip_atomic_fetch_add((LAS unsigned*)lp, 1u, __ATOMIC_RELAXED, __HIP_MEMORY_SCOPE_WORKGROUP);
    if (old == 11u) __hip_atomic_fetch_add((GAS unsigned*)gp, 1u, RLX_AGENT);
  }
}
__global__ __launch_bounds__(768, 3) void k3_ffn(char* __restrict__ ws, u16* __restrict__ out) {
  extern __shared__ __attribute__((aligned(16))) char smem[];
  LAS char* L = (LAS char*)smem;
  const unsigned lds0 = (unsigned)(uintptr_t)L;
  const int tid = threadIdx.x, lane = tid & 63, w = __builtin_amdgcn_readfirstlane(tid >> 6);
#define K3_SIG 9
#define K3_NF 2
#define K3_BARP 100
  const int xcd = blockIdx.x & 7, jx = blockIdx.x >> 3;
  const int fs = (xcd % (8 / K3_NF)) * K3_NF + jx % K3_NF, m = (xcd / (8 / K3_NF)) * (32 / K3_NF) + jx / K3_NF, cg = fs >> 1, jo = fs & 1;
  const unsigned l16 = lane * 16;
  STAMP_INIT();
  STAMP(3, 0);
  const char* hsrc = ws + WS_H2 + (size_t)m * 73728 + ((w & 3) * 18 + (w >> 2)) * 1024;
  const unsigned hdst = lds0 + L3_A + ((w & 3) * 18 + (w >> 2)) * 1024;
#define GA(i) glds16s<((i) & 1) * 3072>(hsrc + ((i) >> 1) * 6144, l16, hdst + ((i) >> 1) * 6144)
  GA(0);
  if (tid == 0) *(LAS u32x4*)(L + L3_FLG) = u32x4{0u, 0u, 0u, 0u};
  constexpr int WIN = K3_WIN, RING = K3_WIN;
  u32x4 wf[RING];
  const __amdgpu_buffer_rsrc_t rw = __builtin_amdgcn_make_buffer_rsrc(ws + WS_WF + (size_t)(fs * 12 + w) * 110592, 0, 110592, 0x00020000);
#define LW(i) { if ((i) < 108) wf[(i) % RING] = __builtin_amdgcn_raw_buffer_load_b128(rw, l16, (i) * 1024, 0); }
  LW(0); LW(1); LW(2);
  SBAR();
  asm volatile("s_waitcnt vmcnt(3) lgkmcnt(0)\n\ts_barrier" ::: "memory");
  SBAR();
  STAMP(3, 1);
  f32x4 acc[3][4];
  u32x4 af[2][4];
  const LAS char* ab = L + L3_A + l16;
#define RA(buf, s) { _Pragma("unroll") for (int j = 0; j < 4; ++j) af[buf][j] = *(const LAS u32x4*)(ab + (j * 18 + (s)) * 1024); }
#define ZACC(i) { _Pragma("unroll") for (int j = 0; j < 4; ++j) acc[i][j] = f32x4{0.f, 0.f, 0.f, 0.f}; }
#define MM(i, fi, buf) { const u32x4 c_ = wf[(fi) % RING]; _Pragma("unroll") for (int j = 0; j < 4; ++j) acc[i][j] = mfma16(c_, af[buf][j], acc[i][j]); }
  ZACC(0); ZACC(1); ZACC(2);
  RA(0, 0);
  GA(1); LW(3); LW(4); LW(5);
  SBAR();
#pragma unroll
  for (int s = 0; s < 18; ++s) {
    if (s == 0) PRIO(3); if (s == 5) PRIO(2); if (s == 10) PRIO(1); if (s == 14) PRIO(0);
    if (s == 2 || s == 5 || s == 8 || (s > 8 && (s - 8) % K3_BARP == 0)) { asm volatile("s_barrier" ::: "memory"); SBAR(); }
    if (s < 17) RA((s + 1) & 1, s + 1);
    if (s == 0) { GA(2); }
    if (s == 1) { GA(3); GA(4); GA(5); }
    SBAR();
#pragma unroll
    for (int i = 0; i < 3; ++i) {
      MM(i, s * 3 + i, s & 1);
      SBAR();
      if (s * 3 + i < WIN - 6) { LW(6 + 2 * (s * 3 + i)); LW(7 + 2 * (s * 3 + i)); } else LW(s * 3 + i + WIN);
      SBAR();
    }
  }
  STAMP(3, 2);
#pragma unroll
  for (int i = 0; i < 3; ++i) {
    const int t = w * 3 + i;
#pragma unroll
    for (int j = 0; j < 4; ++j) {
      const u32x2 o = {relu2(pk2(acc[i][j][0], acc[i][j][1])), relu2(pk2(acc[i][j][2], acc[i][j][3]))};
      *(LAS u32x2*)(L + L3_H + (j * 18 + (t >> 1)) * 1024 + l16 + (t & 1) * 8) = o;
    }
  }
  BAR_L();
  SBAR();
  STAMP(3, 3);
  ab = L + L3_H + l16;
  const __amdgpu_buffer_rsrc_t rs = __builtin_amdgcn_make_buffer_rsrc(ws + WS_PART + (size_t)m * 589824, 0, 589824, 0x00020000);
#define PUB(i, jp, t)                                                                                                        \
  __builtin_amdgcn_raw_buffer_store_b128(                                                                                    \
      u32x4{pk2(acc[i][2 * (jp)][0], acc[i][2 * (jp)][1]), pk2(acc[i][2 * (jp)][2], acc[i][2 * (jp)][3]),                    \
            pk2(acc[i][2 * (jp) + 1][0], acc[i][2 * (jp) + 1][1]), pk2(acc[i][2 * (jp) + 1][2], acc[i][2 * (jp) + 1][3])},   \
      rs, l16, ((fs * 2 + (jp)) * 36 + (t)) * 1024, AUX_SC1)
  char* cnt = ws + WS_CNT + m * 512;
  const int ub = (jo * 36 + 9 * cg + w) * 1024, pb = fs & ~1;
  const unsigned big = 1u << 30;
#define GET(d, k, on) __builtin_amdgcn_raw_buffer_load_b128(rs, (w < 9 && (on)) ? l16 : big, ((pb + 8 - 2 * (d) + (k)) & 7) * 73728 + ub, AUX_SC1)
  const int prow = tid / 18, pc = tid % 18;
  const size_t ooff = (size_t)(m * 64 + jo * 32 + (tid < 576 ? prow : 0)) * 1152 + cg * 288 + pc * 16;
  u32x4 pv[11], xv, gv;
  RA(0, 0);
  SBAR();
#pragma unroll
  for (int r = 0; r < 3; ++r) {
    ZACC(r);
    const int tp = (9 * cg + 36 - 12 * r + w) % 36;
#pragma unroll
    for (int s = 0; s < 18; ++s) {
      if (s == 0) PRIO(3); if (s == 5) PRIO(2); if (s == 10) PRIO(1); if (s == 14) PRIO(0);
      if (r > 0 && s == 0) { asm volatile("s_barrier" ::: "memory"); SBAR(); }
      if (r < 2 || s < 17) RA((s + 1) & 1, (s + 1) % 18);
      SBAR();
      MM(r, 54 + r * 18 + s, s & 1);
      SBAR();
      if (r > 0 && s == 0) PUB(r - 1, 0, tp);
      if (r > 0 && s == 3) PUB(r - 1, 1, tp);
      if (r > 0 && (s == K3_SIG || s == K3_SIG + 2 || s == K3_SIG + 4)) {
        if ((w >> 2) == (s - K3_SIG) / 2) {
          asm volatile("s_waitcnt vmcnt(0)" ::: "memory");
          wg_signal(L + L3_FLG + (r - 1) * 4, cnt + (r - 1) * 128, lane);
          if (r == 2) poll_ge(cnt, 8u);
        }
      }
      if (r == 2 && s == K3_SIG + 4) { pv[0] = GET(3, 0, true); pv[1] = GET(3, 1, true); pv[2] = GET(2, 0, w >= 6); pv[3] = GET(2, 1, w >= 6); }
      if (r == 2 && s == 6) {
        xv = *(const u32x4*)(ws + WS_X1 + ooff);
        gv = *(const u32x4*)(ws + WS_MOD + 5 * 1152 + cg * 288 + pc * 16);
      }
      LW(54 + r * 18 + s + WIN);
      SBAR();
    }
    STAMP(3, 4 + r);
  }
  {
    const int t2 = (9 * cg + w) % 36;
    if (w >= 9 || jo == 1) PUB(2, 0, t2);
    if (w >= 9 || jo == 0) PUB(2, 1, t2);
  }
  asm volatile("s_waitcnt vmcnt(0)" ::: "memory");
  wg_signal(L + L3_FLG + 8, cnt + 256, lane);
  STAMP(3, 7);
  if (w < 9) {
    poll_ge(cnt + 128, 8u);
    pv[4] = GET(2, 0, w < 6); pv[5] = GET(2, 1, w < 6); pv[6] = GET(1, 0, w >= 3); pv[7] = GET(1, 1, w >= 3);
    SBAR();
    lds_wait(L + L3_FLG + 12, 1);
    pv[8] = GET(0, jo ^ 1, true); pv[9] = GET(1, 0, w < 3); pv[10] = GET(1, 1, w < 3);
    f32x4 sa = jo ? acc[2][2] : acc[2][0], sb = jo ? acc[2][3] : acc[2][1];
#pragma unroll
    for (int f = 0; f < 11; ++f) {
      sa += f32x4{bflo(pv[f][0]), bfhi(pv[f][0]), bflo(pv[f][1]), bfhi(pv[f][1])};
      sb += f32x4{bflo(pv[f][2]), bfhi(pv[f][2]), bflo(pv[f][3]), bfhi(pv[f][3])};
    }
    LAS char* tb = L + (lane & 15) * 592 + w * 64 + (lane >> 4) * 16;
    *(LAS f32x4*)tb = sa;
    *(LAS f32x4*)(tb + 16 * 592) = sb;
  } else if (w == 9) {
    poll_ge(cnt + 256, 8u);
    lds_arrive(L + L3_FLG + 12, lane);
  }
  STAMP(3, 8);
  BAR_L();
  if (w < 9) {
    const f32x4 y0 = *(const LAS f32x4*)(L + prow * 592 + pc * 32), y1 = *(const LAS f32x4*)(L + prow * 592 + pc * 32 + 16);
    const u32x4 o = {pk2(fmaf(bflo(gv[0]), y0[0], bflo(xv[0])), fmaf(bfhi(gv[0]), y0[1], bfhi(xv[0]))),
                     pk2(fmaf(bflo(gv[1]), y0[2], bflo(xv[1])), fmaf(bfhi(gv[1]), y0[3], bfhi(xv[1]))),
                     pk2(fmaf(bflo(gv[2]), y1[0], bflo(xv[2])), fmaf(bfhi(gv[2]), y1[1], bfhi(xv[2]))),
                     pk2(fmaf(bflo(gv[3]), y1[2], bflo(xv[3])), fmaf(bfhi(gv[3]), y1[3], bfhi(xv[3])))};
    *(u32x4*)((char*)out + ooff) = o;
  }
  asm volatile("adaln_code_end:");
  STAMP(3, 9);
  STAMP_FLUSH(3);
}

extern "C" void kernel_launch(void* const* d_in, const int* in_sizes, int n_in, void* d_out, int out_size, void* d_ws,
                              size_t ws_size, hipStream_t stream) {
  const u16* x = (const u16*)d_in[0];
  const u16* wq = (const u16*)d_in[1];
  const u16* wk = (const u16*)d_in[2];
  const u16* wv = (const u16*)d_in[3];
  const u16* wo = (const u16*)d_in[4];
  const u16* w1 = (const u16*)d_in[6];
  const u16* w2 = (const u16*)d_in[7];
  const u16* c = (const u16*)d_in[8];
  const u16* wmod = (const u16*)d_in[9];
  char* ws = (char*)d_ws;
  (void)hipFuncSetAttribute((const void*)k2_attn, hipFuncAttributeMaxDynamicSharedMemorySize, L2_BYTES);
  (void)hipFuncSetAttribute((const void*)k3_ffn, hipFuncAttributeMaxDynamicSharedMemorySize, L3_BYTES);
  (void)hipFuncSetAttribute((const void*)k1_qkv, hipFuncAttributeMaxDynamicSharedMemorySize, L1_BYTES);
  k1_qkv<<<256, 768, L1_BYTES, stream>>>(x, wq, wk, wv, wo, c, wmod, ws);
  k2_attn<<<256, 512, L2_BYTES, stream>>>(x, w1, w2, ws);
  k3_ffn<<<256, 768, L3_BYTES, stream>>>(ws, (u16*)d_out);
}
```

```cpp
#include <hip/hip_runtime.h>
#include <hip/hip_bf16.h>
#include <stdint.h>
#include <type_traits>

typedef unsigned short u16;
typedef short bf16x8 __attribute__((ext_vector_type(8)));
typedef short s16x4 __attribute__((ext_vector_type(4)));
typedef float f32x2 __attribute__((ext_vector_type(2)));
typedef float f32x4 __attribute__((ext_vector_type(4)));
typedef float f32x16 __attribute__((ext_vector_type(16)));
typedef unsigned u32x2 __attribute__((ext_vector_type(2)));
typedef unsigned u32x4 __attribute__((ext_vector_type(4)));
typedef __bf16 bf2_t __attribute__((ext_vector_type(2)));
#define LAS __attribute__((address_space(3)))
#define DI __device__ __forceinline__
#define SBAR() __builtin_amdgcn_sched_barrier(0)
#define PIN(x) asm volatile("" : "+v"(x))
#define EX2(v) __builtin_amdgcn_exp2f(v)

constexpr int D_ = 576;
constexpr float QSCALE = 0.23570226039551584f * 1.4426950408889634f;
constexpr float LN_EPS = 1e-5f;

constexpr size_t WS_FW = 0;
constexpr size_t WS_KMAX = 8192;
constexpr size_t WS_EPOCH = 8704;
constexpr size_t WS_MP = 32768;
constexpr size_t WS_ACK2 = 98304;
constexpr size_t WS_CNT = 131072;
constexpr size_t WS_WQ = 262144;
constexpr size_t WS_WO = WS_WQ + 663552;
constexpr size_t WS_WF = WS_WO + 663552;
constexpr size_t WS_QF = WS_WF + 10616832;
constexpr size_t WS_QF2 = WS_QF + 2097152;
constexpr size_t WS_KF = WS_QF2 + 524288;
constexpr size_t WS_KF2 = WS_KF + 65536;
constexpr size_t WS_VF = WS_KF2 + 8192;
constexpr size_t WS_X1 = WS_VF + 90112;
constexpr size_t WS_H2 = WS_X1 + 2359296;
constexpr size_t WS_PART = WS_H2 + 2359296;
constexpr size_t WS_END = WS_PART + 18874368;

DI float bf2f(unsigned b) { return __uint_as_float(b << 16); }
DI float bflo(unsigned v) { return __uint_as_float(v << 16); }
DI float bfhi(unsigned v) { return __uint_as_float(v & 0xffff0000u); }
DI unsigned pk2(float a, float b) { f32x2 v = {a, b}; return __builtin_bit_cast(unsigned, __builtin_convertvector(v, bf2_t)); }
DI float rbf(float a) { return bflo(pk2(a, a)); }
DI float ln_rstd(float var) { return __builtin_amdgcn_rcpf(rbf(__builtin_amdgcn_sqrtf(rbf(rbf(var) + LN_EPS)))); }
DI f32x16 mfma32(bf16x8 a, bf16x8 b, f32x16 c) { return __builtin_amdgcn_mfma_f32_32x32x16_bf16(a, b, c, 0, 0, 0); }
DI f32x16 mfma32k8(s16x4 a, s16x4 b, f32x16 c) { return __builtin_amdgcn_mfma_f32_32x32x8bf16_1k(a, b, c, 0, 0, 0); }
DI f32x4 mfma16(u32x4 a, u32x4 b, f32x4 c) {
  return __builtin_amdgcn_mfma_f32_16x16x32_bf16(__builtin_bit_cast(bf16x8, a), __builtin_bit_cast(bf16x8, b), c, 0, 0, 0);
}
DI void st128(u32x4 d, __amdgpu_buffer_rsrc_t rs, int vo, int so, int) {
  __builtin_amdgcn_raw_buffer_store_b128(d, rs, vo, so, 16);
  asm volatile("s_nop 1" ::"v"(d) : "memory");
}
DI void glds4(const void* g, unsigned lds_base) {
  unsigned sv;
  asm volatile("s_mov_b32 %0, m0\n\ts_mov_b32 m0, %2\n\ts_nop 0\n\tglobal_load_lds_dword %1, off\n\ts_mov_b32 m0, %0"
               : "=&s"(sv) : "v"(g), "s"(lds_base) : "memory");
}
template <int OFF> DI void glds16s(const void* sbase, unsigned voff, unsigned lds_base) {
  unsigned sv;
  asm volatile("s_mov_b32 %0, m0\n\ts_mov_b32 m0, %3\n\ts_nop 0\n\tglobal_load_lds_dwordx4 %1, %2 offset:%4\n\ts_mov_b32 m0, %0"
               : "=&s"(sv) : "v"(voff), "s"(sbase), "s"(lds_base), "i"(OFF) : "memory");
}
template <int CTRL> DI float dppf(float old, float v) {
  return __builtin_bit_cast(float, __builtin_amdgcn_update_dpp(__builtin_bit_cast(int, old), __builtin_bit_cast(int, v), CTRL, 0xf, 0xf, false));
}
DI float wave_sum(float v) {
  v += dppf<0x111>(0.f, v); v += dppf<0x112>(0.f, v); v += dppf<0x114>(0.f, v); v += dppf<0x118>(0.f, v);
  const float a = __builtin_bit_cast(float, __builtin_amdgcn_readlane(__builtin_bit_cast(int, v), 15));
  const float b = __builtin_bit_cast(float, __builtin_amdgcn_readlane(__builtin_bit_cast(int, v), 31));
  const float c = __builtin_bit_cast(float, __builtin_amdgcn_readlane(__builtin_bit_cast(int, v), 47));
  const float d = __builtin_bit_cast(float, __builtin_amdgcn_readlane(__builtin_bit_cast(int, v), 63));
  return (a + b) + (c + d);
}
DI float wave_max_nonneg(float x) {
  unsigned v = __float_as_uint(x);
  asm volatile("s_nop 1\n\tv_max_u32_dpp %0, %0, %0 row_ror:8 row_mask:0xf bank_mask:0xf\n\ts_nop 1\n\t"
               "v_max_u32_dpp %0, %0, %0 row_ror:4 row_mask:0xf bank_mask:0xf\n\ts_nop 1\n\t"
               "v_max_u32_dpp %0, %0, %0 row_ror:2 row_mask:0xf bank_mask:0xf\n\ts_nop 1\n\t"
               "v_max_u32_dpp %0, %0, %0 row_ror:1 row_mask:0xf bank_mask:0xf\n\ts_nop 0" : "+v"(v));
  const unsigned a = __builtin_amdgcn_readlane(v, 0), b = __builtin_amdgcn_readlane(v, 16);
  const unsigned c = __builtin_amdgcn_readlane(v, 32), d = __builtin_amdgcn_readlane(v, 48);
  const unsigned ab = a > b ? a : b, cd = c > d ? c : d;
  return __uint_as_float(ab > cd ? ab : cd);
}
typedef short v4i16_t __attribute__((ext_vector_type(4)));
DI u32x2 tr_read(const LAS char* p) {
  return __builtin_bit_cast(u32x2, __builtin_amdgcn_ds_read_tr16_b64_v4i16((LAS v4i16_t*)p));
}
DI float xor32f(float v) {
  auto r = __builtin_amdgcn_permlane32_swap(__float_as_uint(v), __float_as_uint(v), false, false);
  return __uint_as_float(r[0]) + __uint_as_float(r[1]);
}

#define GAS __attribute__((address_space(1)))
#define RLX_AGENT __ATOMIC_RELAXED, __HIP_MEMORY_SCOPE_AGENT
#define PRIO(n) __builtin_amdgcn_s_setprio(n)
#define BAR_L() asm volatile("s_waitcnt lgkmcnt(0)\n\ts_barrier" ::: "memory")
constexpr int AUX_SC1 = 16;
constexpr unsigned SPIN_CAP = 1u << 16;
constexpr unsigned LSPIN_CAP = 1u << 18;
DI int wf_index(int ph, int fs, int s, int t) {
  if (ph == 0) return (fs * 12 + t / 3) * 108 + s * 3 + t % 3;
  int rho = t - 9 * (fs >> 1);
  if (rho < 0) rho += 36;
  return rho < 12 ? (fs * 12 + rho) * 108 + 90 + s : (fs * 12 + ((rho - 12) >> 1)) * 108 + 54 + s * 2 + (rho & 1);
}
constexpr int K1_REP = 8;
constexpr int K1_STR = 16;
constexpr int LW_SLEEP = 1;
constexpr int K1_XDELAY = 16;
constexpr int K1_DEFER = 70;
constexpr int K1_PRE = 9;
constexpr int K1_SLEEP = 3;
constexpr int L1_H = 0, L1_OUT = 36864, L1_RT = L1_OUT + 2 * 25088, L1_M = L1_RT + 3 * 4608, L1_RED = L1_M + 4608,
              L1_FLG = L1_RED + 2048, L1_DUMMY = L1_FLG + 32, L1_BYTES = L1_DUMMY + 256;
DI f32x4 mod_round(f32x4 v, float one) {
  unsigned a = pk2(v[0], v[1]), b = pk2(v[2], v[3]);
  a = pk2(bflo(a) + one, bfhi(a) + one); b = pk2(bflo(b) + one, bfhi(b) + one);
  return f32x4{bflo(a), bfhi(a), bflo(b), bfhi(b)};
}
DI void lds_arrive(LAS char* p, int lane) {
  asm volatile("" ::: "memory");
  if (lane == 0) __hip_atomic_fetch_add((LAS unsigned*)p, 1u, __ATOMIC_RELAXED, __HIP_MEMORY_SCOPE_WORKGROUP);
}
template <int SL = LW_SLEEP> DI void lds_wait(LAS char* p, unsigned n) {
#pragma nounroll
  for (unsigned spins = 0; spins < LSPIN_CAP; ++spins)
    { if (__builtin_amdgcn_readfirstlane(*(volatile LAS unsigned*)p) >= n) break; __builtin_amdgcn_s_sleep(SL); }
  asm volatile("" ::: "memory");
}
__global__ __launch_bounds__(768, 3) void k1_qkv(const u16* __restrict__ x, const u16* __restrict__ wq, const u16* __restrict__ wk,
                                                 const u16* __restrict__ wv, const u16* __restrict__ wo, const u16* __restrict__ c,
                                                 const u16* __restrict__ wmod, char* __restrict__ ws) {
  extern __shared__ __attribute__((aligned(16))) char smem[];
  LAS char* L = (LAS char*)smem;
  const unsigned lds0 = (unsigned)(uintptr_t)L;
  const int tid = threadIdx.x, lane = tid & 63, w = __builtin_amdgcn_readfirstlane(tid >> 6), b = blockIdx.x;
  {
    unsigned long long pc;
    int len;
    asm volatile("s_getpc_b64 %0\nadaln_code_begin:\n\ts_mov_b32 %1, adaln_code_end-adaln_code_begin" : "=s"(pc), "=s"(len));
    const int off = tid * 128;
    glds4((const char*)pc + (off < len ? off : 0), lds0 + L1_DUMMY);
  }
  const int xcd = b & 7, jx = b >> 3;
  const int T = (xcd >> 1) * 16 + (jx >> 1), g = (xcd & 1) * 2 + (jx & 1);
  const int q = lane >> 4, n16 = lane & 15;
  const unsigned l16 = lane * 16;
  const LAS char* hb0 = L + L1_H + q * 256 + (n16 ^ q) * 16;
  const LAS char* hb1 = L + L1_H + q * 256 + (n16 ^ q ^ 12) * 16;
#define HFRAG(mt, ks) (*(const LAS u32x4*)((((ks) & 1) ? hb1 : hb0) + (mt) * 18432 + (ks) * 1024))
  unsigned tag = 0;
  if (w < 9) {
    u32x4 xv[4];
    unsigned xe[4];
    asm volatile("s_barrier" ::: "memory");
    if (K1_XDELAY) __builtin_amdgcn_s_sleep(K1_XDELAY);
    SBAR();
    {
      const char* xb = (const char*)x + (size_t)(T * 32 + (w & 7) * 4) * 1152;
#pragma unroll
      for (int i = 0; i < 4; ++i) { xv[i] = *(const u32x4*)(xb + i * 1152 + l16); xe[i] = *(const u16*)(xb + i * 1152 + 1024 + lane * 2); }
    }
    SBAR();
    f32x2 xh[4][4];
    float xh8[4];
    if (w < 8) {
#pragma unroll
      for (int i = 0; i < 4; ++i) {
#pragma unroll
        for (int j = 0; j < 4; ++j) xh[i][j] = f32x2{bflo(xv[i][j]), bfhi(xv[i][j])};
        xh8[i] = bflo(xe[i]);
        const f32x2 s2 = (xh[i][0] + xh[i][1]) + (xh[i][2] + xh[i][3]);
        const float mean = wave_sum(s2[0] + s2[1] + xh8[i]) * (1.f / D_);
        xh8[i] -= mean;
        f32x2 q2 = {xh8[i] * xh8[i], 0.f};
#pragma unroll
        for (int j = 0; j < 4; ++j) { xh[i][j] -= mean; q2 = __builtin_elementwise_fma(xh[i][j], xh[i][j], q2); }
        const float rstd = ln_rstd(wave_sum(q2[0] + q2[1]) * (1.f / D_)), dm = (mean - rbf(mean)) * rstd;
        xh8[i] = fmaf(xh8[i], rstd, dm);
#pragma unroll
        for (int j = 0; j < 4; ++j) xh[i][j] = __builtin_elementwise_fma(xh[i][j], f32x2{rstd, rstd}, f32x2{dm, dm});
      }
#pragma unroll
      for (int i = 0; i < 4; ++i) { PIN(xh8[i]); PIN(xh[i][0]); PIN(xh[i][1]); PIN(xh[i][2]); PIN(xh[i][3]); }
    }
    lds_wait(L + L1_FLG, 1);
    u32x4 wf[18];
    {
      const __amdgpu_buffer_rsrc_t rq = __builtin_amdgcn_make_buffer_rsrc(ws + WS_WQ + (size_t)(g * 9 + w) * 18432, 0, 18432, 0x00020000);
#pragma unroll
      for (int s = 0; s < K1_PRE; ++s) wf[s] = __builtin_amdgcn_raw_buffer_load_b128(rq, l16, s * 1024, AUX_SC1);
    }
    SBAR();
    if (w < 8) lds_wait(L + L1_FLG + 16, 1);
    if (w < 8) {
      const LAS char* mp = L + L1_M;
      const f32x4 s0 = *(const LAS f32x4*)(mp + lane * 32), s1 = *(const LAS f32x4*)(mp + lane * 32 + 16);
      const f32x4 c0 = *(const LAS f32x4*)(mp + 2304 + lane * 32), c1 = *(const LAS f32x4*)(mp + 2304 + lane * 32 + 16);
      const float s8 = *(const LAS float*)(mp + 2048 + lane * 4), c8 = *(const LAS float*)(mp + 2304 + 2048 + lane * 4);
      const int sz = (lane & 3) | ((lane >> 2) & 1) * 12, sz8 = ((lane >> 3) & 3) | (lane >> 5) * 12;
      LAS char* hp = L + L1_H + (w >> 2) * 18432;
#pragma unroll
      for (int i = 0; i < 4; ++i) {
        const int tok = (w & 3) * 4 + i;
        const u32x4 o = {pk2(fmaf(xh[i][0][0], c0[0], s0[0]), fmaf(xh[i][0][1], c0[1], s0[1])),
                         pk2(fmaf(xh[i][1][0], c0[2], s0[2]), fmaf(xh[i][1][1], c0[3], s0[3])),
                         pk2(fmaf(xh[i][2][0], c1[0], s1[0]), fmaf(xh[i][2][1], c1[1], s1[1])),
                         pk2(fmaf(xh[i][3][0], c1[2], s1[2]), fmaf(xh[i][3][1], c1[3], s1[3]))};
        *(LAS u32x4*)(hp + lane * 256 + (tok ^ sz) * 16) = o;
        *(LAS u16*)(hp + 16384 + (lane >> 3) * 256 + (tok ^ sz8) * 16 + (lane & 7) * 2) = (u16)pk2(fmaf(xh8[i], c8, s8), 0.f);
      }
    }
    lds_arrive(L + L1_FLG + 8, lane);
    {
      const __amdgpu_buffer_rsrc_t rq = __builtin_amdgcn_make_buffer_rsrc(ws + WS_WQ + (size_t)(g * 9 + w) * 18432, 0, 18432, 0x00020000);
#pragma unroll
      for (int s = K1_PRE; s < 18; ++s) wf[s] = __builtin_amdgcn_raw_buffer_load_b128(rq, l16, s * 1024, AUX_SC1);
    }
    SBAR();
    lds_wait(L + L1_FLG + 8, 9);
    f32x4 acc[4] = {};
    u32x4 hf[10];
#pragma unroll
    for (int i = 0; i < 10; ++i) hf[i] = HFRAG(i & 1, i >> 1);
    SBAR();
#pragma unroll
    for (int i = 0; i < 36; ++i) {
      acc[i & 3] = mfma16(wf[i >> 1], hf[i % 10], acc[i & 3]);
      SBAR();
      if (i + 10 < 36) hf[i % 10] = HFRAG(i & 1, (i + 10) >> 1);
      SBAR();
    }
#pragma unroll
    for (int mt = 0; mt < 2; ++mt) *(LAS f32x4*)(L + L1_OUT + (mt * 16 + n16) * 784 + w * 64 + q * 16) = acc[mt] + acc[mt + 2];
    lds_arrive(L + L1_FLG + 12, lane);
  } else {
    const int sw = w - 9;
#define LOAD_TAG() tag = __hip_atomic_load((GAS unsigned*)(ws + WS_EPOCH), RLX_AGENT)
#define MAKE_TAG() tag += 0x9E3779B9u
    auto gemv = [&](int un, unsigned epoch, auto first) {
      const int cb = un >> 2, rq = un & 3;
      u32x4 mv[9];
      unsigned cv[9];
      const int r0 = rq * 144 + sw * 8 + (lane >> 3);
      const u16* mp = wmod + (size_t)r0 * (6 * D_) + cb * 64 + (lane & 7) * 8;
#pragma unroll
      for (int i = 0; i < 9; ++i) cv[i] = c[r0 + 16 * i];
#pragma unroll
      for (int i = 0; i < 9; ++i) mv[i] = *(const u32x4*)(mp + (size_t)i * 16 * (6 * D_));
      SBAR();
      if (first.value) { BAR_L(); SBAR(); PRIO(3); }
      float sc[9];
#pragma unroll
      for (int i = 0; i < 9; ++i) {
        const float cf = bf2f(cv[i]);
        sc[i] = rbf(cf * __builtin_amdgcn_rcpf(1.f + EX2(-1.4426950408889634f * cf)));
      }
      SBAR();
      f32x2 acc[4] = {};
#pragma unroll
      for (int i = 0; i < 9; ++i) {
#pragma unroll
        for (int j = 0; j < 4; ++j) acc[j] = __builtin_elementwise_fma(f32x2{sc[i], sc[i]}, f32x2{bflo(mv[i][j]), bfhi(mv[i][j])}, acc[j]);
        SBAR();
      }
      asm volatile("s_nop 1\n\t"
                   "v_add_f32_dpp %0, %0, %0 row_ror:8 row_mask:0xf bank_mask:0xf\n\tv_add_f32_dpp %1, %1, %1 row_ror:8 row_mask:0xf bank_mask:0xf\n\t"
                   "v_add_f32_dpp %2, %2, %2 row_ror:8 row_mask:0xf bank_mask:0xf\n\tv_add_f32_dpp %3, %3, %3 row_ror:8 row_mask:0xf bank_mask:0xf\n\t"
                   "v_add_f32_dpp %4, %4, %4 row_ror:8 row_mask:0xf bank_mask:0xf\n\tv_add_f32_dpp %5, %5, %5 row_ror:8 row_mask:0xf bank_mask:0xf\n\t"
                   "v_add_f32_dpp %6, %6, %6 row_ror:8 row_mask:0xf bank_mask:0xf\n\tv_add_f32_dpp %7, %7, %7 row_ror:8 row_mask:0xf bank_mask:0xf"
                   : "+v"(acc[0][0]), "+v"(acc[0][1]), "+v"(acc[1][0]), "+v"(acc[1][1]), "+v"(acc[2][0]), "+v"(acc[2][1]), "+v"(acc[3][0]),
                     "+v"(acc[3][1]));
      if (sw == 0 && epoch > 1) lds_wait(L + L1_FLG + 20, epoch - 1);
      if ((lane & 8) == 0) {
        LAS char* rp = L + L1_RED + (sw * 4 + q) * 256 + (lane & 7) * 32;
        *(LAS f32x4*)rp = f32x4{acc[0][0], acc[0][1], acc[1][0], acc[1][1]};
        *(LAS f32x4*)(rp + 16) = f32x4{acc[2][0], acc[2][1], acc[3][0], acc[3][1]};
      }
      if (sw == 0) {
        lds_arrive(L + L1_FLG + 4, lane);
      } else {
        lds_wait(L + L1_FLG + 4, epoch);
        float pv[8];
#pragma unroll
        for (int i = 0; i < 8; ++i) pv[i] = *(const LAS float*)(L + L1_RED + i * 256 + lane * 4);
        const float s = ((pv[0] + pv[1]) + (pv[2] + pv[3])) + ((pv[4] + pv[5]) + (pv[6] + pv[7]));
        __hip_atomic_store((GAS float*)(ws + WS_MP + (rq * 3456 + cb * 64 + lane) * 4), s, RLX_AGENT);
        asm volatile("s_waitcnt vmcnt(0)" ::: "memory");
        lds_arrive(L + L1_FLG + 20, lane);
      }
      PRIO(0);
    };
    u32x4 fe[9], fo[9];
    if (tid >= 704 && tid < 706) *(LAS u32x4*)(L + L1_FLG + (tid & 1) * 16) = u32x4{0u, 0u, 0u, 0u};
    const int pi = jx * 4 + (xcd >> 1);
    if (sw == 2) {
      const int uq = g < 2 ? pi : 255 - pi;
      const bool rt = uq < 162;
      const int ks = uq / 9, nb = uq % 9;
      u32x4 rv[4];
      const __amdgpu_buffer_rsrc_t ro = __builtin_amdgcn_make_buffer_rsrc((void*)wq, 0, rt ? 576 * 1152 : 0, 0x00020000);
#pragma unroll
      for (int i = 0; i < 4; ++i)
        rv[i] = __builtin_amdgcn_raw_buffer_load_b128(ro, (lane >> 3) * 1152 + (lane & 7) * 16, (ks * 32 + i * 8) * 1152 + nb * 128, 0);
      SBAR();
      LOAD_TAG();
      SBAR();
      BAR_L();
      SBAR();
      PRIO(3);
      MAKE_TAG();
      LAS char* R = L + L1_RT + 2 * 4608;
#pragma unroll
      for (int i = 0; i < 4; ++i) *(LAS u32x4*)(R + (i * 8 + (lane >> 3)) * 144 + (lane & 7) * 16) = rv[i];
      asm volatile("s_waitcnt lgkmcnt(0)" ::: "memory");
      const LAS char* tp = R + (8 * q + ((lane >> 2) & 3)) * 144 + (lane & 3) * 8;
      const __amdgpu_buffer_rsrc_t rd = __builtin_amdgcn_make_buffer_rsrc(ws + WS_WQ, 0, rt ? 663552 : 0, 0x00020000);
#pragma unroll
      for (int t = 0; t < 4; ++t) {
        const u32x2 lo = tr_read(tp + t * 32), hi = tr_read(tp + 4 * 144 + t * 32);
        st128(u32x4{lo[0], lo[1], hi[0], hi[1]}, rd, l16, ((nb * 4 + t) * 18 + ks) * 1024, AUX_SC1);
      }
      asm volatile("s_waitcnt vmcnt(0)" ::: "memory");
      if (g >= 2 && pi < 72) lds_wait(L + L1_FLG + 20, 1);
      if (lane < K1_REP) __hip_atomic_store((GAS unsigned*)(ws + WS_ACK2 + lane * 256 * K1_STR + b * K1_STR), tag, RLX_AGENT);
    } else if (g >= 2) {
      if (pi < 72) gemv(pi, 1u, std::true_type{});
      else BAR_L();
    } else {
      BAR_L();
      const __amdgpu_buffer_rsrc_t rw = __builtin_amdgcn_make_buffer_rsrc((void*)(g == 0 ? wk : wv), 0, 576 * 36, 0x00020000);
      const int vo = (sw * 32 + 8 * q) * 36 + n16 * 4;
#pragma unroll
      for (int s = 0; s < 9; ++s)
#pragma unroll
        for (int j = 0; j < 4; ++j) {
          const int r0 = 64 * s + 2 * j;
          const unsigned a0 = __builtin_amdgcn_raw_buffer_load_b32(rw, vo, r0 * 36, 0);
          const unsigned a1 = __builtin_amdgcn_raw_buffer_load_b32(rw, vo, (r0 + 1) * 36, 0);
          fe[s][j] = __builtin_amdgcn_perm(a1, a0, 0x05040100u);
          fo[s][j] = __builtin_amdgcn_perm(a1, a0, 0x07060302u);
        }
    }
    if (w == 11) {
      *(u32x2*)(ws + WS_CNT + b * 512 + lane * 8) = u32x2{0u, 0u};
      if (lane < 2) *(unsigned*)(ws + WS_FW + b * 8 + lane * 4) = 0u;
      const __amdgpu_buffer_rsrc_t rg = __builtin_amdgcn_make_buffer_rsrc(ws + WS_ACK2 + (b % K1_REP) * 256 * K1_STR, 0, 256 * K1_STR, 0x00020000);
      for (unsigned spins = 0; spins < SPIN_CAP; ++spins) {
        asm volatile("" ::: "memory");
        u32x4 ak;
        if (K1_STR == 4) ak = __builtin_amdgcn_raw_buffer_load_b128(rg, l16, 0, AUX_SC1);
        else for (int i = 0; i < 4; ++i) ak[i] = __builtin_amdgcn_raw_buffer_load_b32(rg, lane * K1_STR, i * 64 * K1_STR, AUX_SC1);
        if (__all(ak[0] == tag && ak[1] == tag && ak[2] == tag && ak[3] == tag)) break;
        __builtin_amdgcn_s_sleep(K1_SLEEP);
      }
      __builtin_amdgcn_fence(__ATOMIC_ACQUIRE, "wavefront");
      const __amdgpu_buffer_rsrc_t rm = __builtin_amdgcn_make_buffer_rsrc(ws + WS_MP, 0, 4 * 13824, 0x00020000);
      u32x4 pm[5][4];
#pragma unroll
      for (int i = 0; i < 5; ++i)
#pragma unroll
        for (int k = 0; k < 4; ++k) pm[i][k] = __builtin_amdgcn_raw_buffer_load_b128(rm, l16, k * 13824 + i * 1024, AUX_SC1);
      SBAR();
      lds_arrive(L + L1_FLG, lane);
      SBAR();
#pragma unroll
      for (int i = 0; i < 5; ++i) {
        const f32x4 sm = (__builtin_bit_cast(f32x4, pm[i][0]) + __builtin_bit_cast(f32x4, pm[i][1])) +
                         (__builtin_bit_cast(f32x4, pm[i][2]) + __builtin_bit_cast(f32x4, pm[i][3]));
        const float one = i * 64 + lane >= 144 ? 1.f : 0.f;
        if (i < 4 || lane < 32) *(LAS f32x4*)(L + L1_M + i * 1024 + l16) = mod_round(sm, one);
      }
      lds_arrive(L + L1_FLG + 16, lane);
      if (b == 0 && lane == 0) __hip_atomic_store((GAS unsigned*)(ws + WS_EPOCH), tag, RLX_AGENT);
    }
    if (g < 2 && sw < 2) {
      lds_wait(L + L1_FLG + 8, 9);
      f32x4 ae[2] = {}, ao[2] = {};
      u32x4 hf[18];
#pragma unroll
      for (int i = 0; i < 18; ++i) hf[i] = *(const LAS u32x4*)((sw ? hb1 : hb0) + (i & 1) * 18432 + (2 * (i >> 1) + sw) * 1024);
      SBAR();
#pragma unroll
      for (int i = 0; i < 18; ++i) {
        ae[i & 1] = mfma16(fe[i >> 1], hf[i], ae[i & 1]);
        ao[i & 1] = mfma16(fo[i >> 1], hf[i], ao[i & 1]);
      }
#pragma unroll
      for (int mt = 0; mt < 2; ++mt) {
        LAS char* op = L + L1_OUT + (sw * 32 + mt * 16 + n16) * 784 + 640 + q * 32;
        *(LAS f32x4*)op = f32x4{ae[mt][0], ao[mt][0], ae[mt][1], ao[mt][1]};
        *(LAS f32x4*)(op + 16) = f32x4{ae[mt][2], ao[mt][2], ae[mt][3], ao[mt][3]};
      }
      lds_arrive(L + L1_FLG + 12, lane);
    } else if (g >= 2) {
      if (pi >= 72 && sw < 2) __builtin_amdgcn_s_sleep(K1_DEFER);
      const int uu = pi + 128 * sw;
      const bool rt = sw < 2 && uu < 162;
      const int rs_ = uu / 9, nb = uu % 9, st = rs_ >> 1, kk = rs_ & 1;
      u32x4 rv[4];
      {
        const int k0 = st < 8 ? 72 * st + 32 * kk : 288 * kk + 64, kq = st < 8 ? 8 : 72;
        const __amdgpu_buffer_rsrc_t ro = __builtin_amdgcn_make_buffer_rsrc((void*)wo, 0, rt ? 576 * 1152 : 0, 0x00020000);
#pragma unroll
        for (int i = 0; i < 4; ++i)
          rv[i] = __builtin_amdgcn_raw_buffer_load_b128(ro, (lane >> 3) * 1152 + (lane & 7) * 16, (k0 + i * kq) * 1152 + nb * 128, 0);
      }
      SBAR();
      if (sw < 2) {
        unsigned ep = pi < 72 ? 2u : 1u;
        for (int un = pi < 72 ? 184 + pi : pi; un < (pi < 72 ? 216 : 184); un += 56) gemv(un, ep++, std::false_type{});
      }
      PIN(rv[0]); PIN(rv[1]); PIN(rv[2]); PIN(rv[3]);
      if (rt) {
        LAS char* R = L + L1_RT + sw * 4608;
#pragma unroll
        for (int i = 0; i < 4; ++i) *(LAS u32x4*)(R + (i * 8 + (lane >> 3)) * 144 + (lane & 7) * 16) = rv[i];
        asm volatile("s_waitcnt lgkmcnt(0)" ::: "memory");
        const LAS char* tp = R + (8 * q + ((lane >> 2) & 3)) * 144 + (lane & 3) * 8;
#pragma unroll
        for (int t = 0; t < 4; ++t) {
          const int tt = nb * 4 + t;
          const u32x2 lo = tr_read(tp + t * 32), hi = tr_read(tp + 4 * 144 + t * 32);
          *(u32x4*)(ws + WS_WO + (size_t)(((st * 4 + tt / 9) * 2 + kk) * 9 + tt % 9) * 1024 + lane * 16) = u32x4{lo[0], lo[1], hi[0], hi[1]};
        }
      }
      return;
    }
  }
  if (w < 10) lds_wait(L + L1_FLG + 12, g < 2 ? 11 : 9);
  const int r = lane & 31, h = lane >> 5;
  if (w < 8 || (w == 8 && g == 0)) {
    const bool isq = w < 8;
    const int tok = isq ? (w >> 1) * 8 + (r & 7) : r, col = isq ? ((w & 1) * 4 + (r >> 3)) * 18 : 160;
    const LAS char* ip = L + L1_OUT + tok * 784 + col * 4;
    f32x2 v[5];
#pragma unroll
    for (int j = 0; j < 4; ++j) v[j] = *(const LAS f32x2*)(ip + h * 32 + j * 8);
    v[4] = *(const LAS f32x2*)(ip + 64);
    if (!isq) {
#pragma unroll
      for (int j = 0; j < 4; ++j) v[j] += *(const LAS f32x2*)(ip + 25088 + h * 32 + j * 8);
      v[4] += *(const LAS f32x2*)(ip + 25088 + 64);
    }
    const float sc = isq ? QSCALE : 1.f;
    const u32x4 o = {pk2(v[0][0] * sc, v[0][1] * sc), pk2(v[1][0] * sc, v[1][1] * sc), pk2(v[2][0] * sc, v[2][1] * sc),
                     pk2(v[3][0] * sc, v[3][1] * sc)};
    const unsigned o2 = h == 0 ? pk2(v[4][0] * sc, v[4][1] * sc) : 0u;
    float n2 = bflo(o2) * bflo(o2) + bfhi(o2) * bfhi(o2);
#pragma unroll
    for (int j = 0; j < 4; ++j) n2 = fmaf(bflo(o[j]), bflo(o[j]), fmaf(bfhi(o[j]), bfhi(o[j]), n2));
    n2 = xor32f(n2);
    if (isq) {
      const size_t fi = (size_t)(T * 4 + (w >> 1)) * 8 + g * 2 + (w & 1);
      *(u32x4*)(ws + WS_QF + fi * 1024 + lane * 16) = o;
      const unsigned nb = (__float_as_uint(__builtin_amdgcn_sqrtf(n2) * 1.0001f) + 0xffffu) >> 16;
      *(unsigned*)(ws + WS_QF2 + fi * 256 + lane * 4) = h == 0 ? o2 : nb;
    } else {
      *(u32x4*)(ws + WS_KF + (size_t)T * 1024 + lane * 16) = o;
      if (h == 0) *(unsigned*)(ws + WS_KF2 + (((T >> 3) * 32 + r) * 8 + (T & 7)) * 4) = o2;
      n2 = wave_max_nonneg(n2);
      if (lane == 0) *(float*)(ws + WS_KMAX + T * 4) = n2;
    }
  } else if (w < 10 && g == 1) {
    const int s = w - 8;
    const LAS char* ip = L + L1_OUT + (16 * s + 4 * h) * 784 + (160 + r) * 4;
    float v[8];
#pragma unroll
    for (int j = 0; j < 8; ++j) {
      const int to = (8 * (j >> 2) + (j & 3)) * 784;
      v[j] = *(const LAS float*)(ip + to) + *(const LAS float*)(ip + 25088 + to);
    }
    u32x4 o = {pk2(v[0], v[1]), pk2(v[2], v[3]), pk2(v[4], v[5]), pk2(v[6], v[7])};
    if (r >= 18) { const unsigned cc = r == 18 ? 0x3f803f80u : 0u; o = u32x4{cc, cc, cc, cc}; }
    if (r < 21) *(u32x4*)(ws + WS_VF + (T >> 3) * 11264 + ((((T & 7) * 2 + s) * 2 + h) * 21 + r) * 16) = o;
  }
}

constexpr int L2_RT = 0, L2_V = 16384, L2_PB = L2_V + 90112, L2_OB = L2_PB + 22272, L2_Q = L2_OB + 9216, L2_Q2 = L2_Q + 8192, L2_X = L2_Q2 + 2048,
              L2_M = L2_X + 9216, L2_SY = L2_M + 4096, L2_SP = L2_SY + 64, L2_BYTES = L2_SP + 2048;
static_assert(L2_BYTES <= 163840, "LDS");
constexpr int NVK = 5;

DI int ob_off(int g, int cl, int tok) {
  return cl < 64 ? g * 1024 + (cl >> 3) * 128 + tok * 16 + (cl & 7) * 2 : 8192 + g * 128 + tok * 16 + (cl - 64) * 2;
}
#define LP(T, a) ((LAS T*)(uintptr_t)(a))

typedef short s16x2 __attribute__((ext_vector_type(2)));
DI unsigned relu2(unsigned v) {
  return __builtin_bit_cast(unsigned, __builtin_elementwise_max(__builtin_bit_cast(s16x2, v), s16x2{0, 0}));
}
DI void poll_flags(char* p, int lane) {
#pragma nounroll
  for (unsigned spins = 0; spins < SPIN_CAP; ++spins) {
    if (__all(__hip_atomic_load((GAS unsigned*)(p + (lane & 7) * 16), RLX_AGENT) != 0u)) break;
    __builtin_amdgcn_s_sleep(1);
  }
  __builtin_amdgcn_fence(__ATOMIC_ACQUIRE, "wavefront");
}
DI void wg_signal(LAS char* lp, char* gp, int lane, unsigned last) {
  unsigned old = 0;
  if (lane == 0) old = __hip_atomic_fetch_add((LAS unsigned*)lp, 1u, __ATOMIC_RELAXED, __HIP_MEMORY_SCOPE_WORKGROUP);
  if (__builtin_amdgcn_readfirstlane(old) == last && lane < 8) __hip_atomic_store((GAS unsigned*)(gp + lane * 512), 1u, RLX_AGENT);
}
DI void poll_line(char* p, int lane) {
#pragma nounroll
  for (unsigned spins = 0; spins < SPIN_CAP; ++spins) {
    if (__all(__hip_atomic_load((GAS unsigned*)(p + (lane & 31) * 4), RLX_AGENT) != 0u)) break;
    __builtin_amdgcn_s_sleep(2);
  }
  __builtin_amdgcn_fence(__ATOMIC_ACQUIRE, "wavefront");
}
constexpr int PRE1 = 15;
constexpr int K3_SIG = 13;
constexpr int K3_NF = 2;
constexpr int L3_A = 0, L3_H = 73728, L3_FLG = L2_SY + 32;
__global__ __launch_bounds__(768, 3) void k2_attn(const u16* __restrict__ x, const u16* __restrict__ w1, const u16* __restrict__ w2,
                                                  char* __restrict__ ws, u16* __restrict__ out) {
  extern __shared__ __attribute__((aligned(16))) char smem[];
  LAS char* L = (LAS char*)smem;
  const unsigned lds0 = (unsigned)(uintptr_t)L;
  const int tid = threadIdx.x, lane = tid & 63, w = __builtin_amdgcn_readfirstlane(tid >> 6);
  const int xcd = blockIdx.x & 7, jx = blockIdx.x >> 3;
  const int fs = (xcd % (8 / K3_NF)) * K3_NF + jx % K3_NF, m = (xcd / (8 / K3_NF)) * (32 / K3_NF) + jx / K3_NF, cg = fs >> 1, jo = fs & 1;
  const int b = m * 8 + fs;
  const unsigned l16 = lane * 16;
  if (w < 8) {
    const int r = lane & 31, h = lane >> 5;
    bf16x8 Kf[8], Vr[2], Vk[NVK + 1];
    const char* kp = ws + WS_KF + (size_t)w * 8192;
#define ALD4(dst, vo, sb, off) asm volatile("global_load_dwordx4 %0, %1, %2 offset:%3" : "=v"(dst) : "v"(vo), "s"(sb), "i"(off) : "memory")
#define ALD1(dst, vo, sb) asm volatile("global_load_dword %0, %1, %2" : "=v"(dst) : "v"(vo), "s"(sb) : "memory")
#define WV(n, x) asm volatile("s_waitcnt vmcnt(" #n ")" : "+v"(x)::"memory")
#define LK(t) ALD4(Kf[t], l16, kp + ((t) >> 2) * 4096, ((t) & 3) * 1024)
#define GV(i) glds16s<((i) & 3) * 1024>(ws + WS_VF + w * 11264 + ((i) >> 2) * 4096, l16, lds0 + L2_V + w * 11264 + ((i) >> 2) * 4096)
    float km_in;
    unsigned q2;
    u32x4 K2a, K2b;
    bf16x8 Q1;
    ALD1(km_in, lane * 4, ws + WS_KMAX);
    ALD1(q2, lane * 4, ws + WS_QF2 + (size_t)b * 2048);
    ALD4(K2a, r * 32, ws + WS_KF2 + w * 1024, 0);
    ALD4(Q1, l16, ws + WS_QF + (size_t)b * 8192, 0);
    LK(0);
    SBAR();
    asm volatile("s_barrier" ::: "memory");
    SBAR();
    GV(0); GV(1); LK(1); GV(2); GV(3); LK(2);
    ALD4(K2b, r * 32, ws + WS_KF2 + w * 1024, 16);
    SBAR();
    const int rr = (w & 3) * 8 + (lane >> 3);
    unsigned rvo1 = ((w >> 2) * 32 + 8 * ((rr >> 2) & 3) + 4 * (rr >> 4) + (rr & 3)) * 9216 + (lane & 7) * 16;
    unsigned rvo2 = ((w >> 2) * 32 + rr) * 1152 + (lane & 7) * 16;
    unsigned rra = lds0 + L2_RT + ((w >> 2) * 32 + (lane >> 2)) * 128 + (w & 3) * 32 + (lane & 3) * 8;
    const __amdgpu_buffer_rsrc_t rwf = __builtin_amdgcn_make_buffer_rsrc(ws + WS_WF, 0, (int)(WS_END - WS_WF), 0x00020000);
    int rsrcv, rdstv;
    PIN(rvo1); PIN(rvo2); PIN(rra);
#define RTAB() { const u32x2 t_ = *(const LAS u32x2*)(L + L2_SP + w * 64 + (lane & 7) * 8); rsrcv = t_[0]; rdstv = t_[1]; }
    u32x2 rlo = {0u, 0u}, rhi = {0u, 0u};
#define rt_load(i) glds16s<0>((const char*)((i) < 3 ? w1 : w2) + __builtin_amdgcn_readlane(rsrcv, i), (i) < 3 ? rvo1 : rvo2, \
                              lds0 + L2_RT + ((i) & 1) * 8192 + w * 1024)
#define rt_store(i) st128(u32x4{rlo[0], rlo[1], rhi[0], rhi[1]}, rwf, l16, __builtin_amdgcn_readlane(rdstv, i), AUX_SC1)
#define RTR() { rlo = tr_read(LP(const char, rra)); rhi = tr_read(LP(const char, rra + 2048)); rra ^= 8192; PIN(rra); }
    SBAR();
    unsigned nkk;
    {
      WV(11, km_in);
      const float km = wave_max_nonneg(km_in);
      nkk = ((__float_as_uint(__builtin_amdgcn_sqrtf(km) * 1.0001f) + 0xffffu) >> 16) | 0x8000u;
      PIN(nkk);
      WV(9, K2a); WV(9, q2);
#pragma unroll
      for (int t = 0; t < 4; ++t) K2a[t] = h == 0 ? K2a[t] : nkk;
    }
    const f32x16 zero16 = {};
    f32x16 O, Oold, SA, SB;
    u32x2 qa = {q2, 0u}, qb = {0u, q2};
    u32x4 Plo, Phi = {0u, 0u, 0u, 0u};
    float e14 = 0.f, e15 = 0.f;
    unsigned vra = lds0 + L2_V + w * 11264 + (h * 21 + (r < 19 ? r : (r >= 20 && r < 28 ? 20 : 19))) * 16;
    unsigned pbw = lds0 + L2_PB + w * 2720 + r * 16 + h * 544, pbw2 = lds0 + L2_PB + (h ? 21760 : w * 2720 + 2176) + r * 16;
    unsigned qra = lds0 + L2_Q + l16 + 1024, q2a = lds0 + L2_Q2 + lane * 4 + 256, sya = lds0 + L2_SY, fv;
    PIN(vra); PIN(pbw); PIN(pbw2); PIN(qra); PIN(q2a); PIN(sya);
#define KPAIR(t) __builtin_bit_cast(s16x4, (t) < 4 ? u32x2{K2a[(t) & 2], K2a[((t) & 2) + 1]} : u32x2{K2b[(t) & 2], K2b[((t) & 2) + 1]})
#define QPAIR(t) __builtin_bit_cast(s16x4, ((t) & 1) ? qb : qa)
#define BF8(x) __builtin_bit_cast(bf16x8, x)
#define VIX(i) (((i) + 1) & 15)
#define VKEEP(i) (VIX(i) < NVK)
#define RV(slot, i) { if (!VKEEP(i)) Vr[slot] = *LP(const bf16x8, vra + ((i) & 15) * 672);                          \
                      else if (FIRST && (i) < 16) Vk[VKEEP(i) ? VIX(i) : 0] = *LP(const bf16x8, vra + ((i) & 15) * 672); }
#define VOP(slot, i) (VKEEP(i) ? Vk[VKEEP(i) ? VIX(i) : 0] : Vr[slot])
    SA = mfma32k8(KPAIR(0), QPAIR(0), zero16);
    WV(7, Kf[0]); WV(7, Q1);
    SA = mfma32(Kf[0], Q1, SA);
#define EEC(cur, i, P, j)                                                                               \
  {                                                                                                     \
    float a_ = EX2(cur[i]), b_ = EX2(cur[(i) + 1]);                                                     \
    PIN(a_); PIN(b_); SBAR();                                                                           \
    P[j] = pk2(e14, e15); PIN(P[j]); SBAR();                                                            \
    e14 = a_; e15 = b_;                                                                                 \
  }
#define TILE(t, cur, nxt, S0, S1, S2, S3)                                                               \
  {                                                                                                     \
    nxt = mfma32k8(KPAIR(((t) + 1) & 7), QPAIR(((t) + 1) & 7), zero16); SBAR();                         \
    EEC(cur, 0, Phi, 3); EEC(cur, 2, Plo, 0); S0; SBAR();                                               \
    nxt = mfma32(Kf[((t) + 1) & 7], Q1, nxt); SBAR();                                                   \
    if (FIRST && (t) == 0) RV(1, 0);                                                                    \
    SBAR();                                                                                             \
    EEC(cur, 4, Plo, 1); EEC(cur, 6, Plo, 2); S1; SBAR();                                               \
    if ((t) != 0) O = mfma32(VOP(0, 2 * (t) + 15), BF8(Phi), O);                                                       \
    else if (!FIRST) asm volatile("v_mfma_f32_32x32x16_bf16 %0, %1, %2, %3" : "=&v"(Oold) : "v"(VOP(0, 15)), "v"(Phi), "v"(O)); \
    SBAR();                                                                                             \
    EEC(cur, 8, Plo, 3); RV(0, 2 * (t) + 1); SBAR(); EEC(cur, 10, Phi, 0); S2; SBAR();                  \
    if ((t) == 0 && !FIRST) asm volatile("" ::"v"(O));                                                  \
    O = mfma32(VOP(1, 2 * (t)), BF8(Plo), (t) == 0 ? zero16 : O);                                                 \
    SBAR();                                                                                             \
    EEC(cur, 12, Phi, 1); RV(1, 2 * (t) + 2); SBAR(); EEC(cur, 14, Phi, 2); S3; SBAR();                 \
  }
#define WP0() { PIN(Oold); *LP(f32x4, pbw) = f32x4{Oold[0], Oold[1], Oold[2], Oold[3]}; }
#define WP1() { *LP(f32x4, pbw + 1088) = f32x4{Oold[4], Oold[5], Oold[6], Oold[7]}; }
#define WP2() { *LP(f32x4, pbw2) = f32x4{Oold[8], Oold[9], Oold[10], Oold[11]}; }
#define POST(o) asm volatile("s_mov_b64 exec, 1\n\tds_add_u32 %0, %1 offset:" #o "\n\ts_mov_b64 exec, -1" ::"v"(sya), "v"(one) : "memory")
#define FLAGRD(o) { fv = *(volatile LAS unsigned*)(uintptr_t)(sya + (o)); }
#define CHECK(o, n) { unsigned c_;                                                                                           \
    asm volatile("s_mov_b32 %1, 0x40000\n1:\n\tv_cmp_gt_u32 vcc, %2, %0\n\ts_cbranch_vccz 2f\n\tds_read_b32 %0, %3 offset:" #o "\n\t" \
                 "s_sleep 1\n\ts_sub_u32 %1, %1, 1\n\ts_waitcnt lgkmcnt(0)\n\ts_cbranch_scc0 1b\n2:"                        \
                 : "+v"(fv), "=&s"(c_) : "s"(n), "v"(sya) : "vcc", "scc", "memory"); }
#define RQA() { qa[0] = *LP(const unsigned, q2a); }
#define RQB() { qb[1] = *LP(const unsigned, q2a); }
#define RQ1() { Q1 = *LP(const bf16x8, qra); }
#define QINC() { qra += 1024; q2a += 256; PIN(qra); PIN(q2a); }
#define FIXK() { _Pragma("unroll") for (int t = 0; t < 4; ++t) { K2b[t] = h == 0 ? K2b[t] : nkk; } PIN(K2b); }
#define NOP {}
    unsigned one = 1;
    PIN(one);
    {
      constexpr bool FIRST = true;
      PRIO(2);
      TILE(0, SA, SB, { GV(4); LK(3); WV(6, Kf[1]); }, { GV(5); LK(4); }, { GV(6); GV(7); }, { LK(5); GV(8); });
      TILE(1, SB, SA, { LK(6); GV(9); WV(11, Kf[2]); }, { GV(10); LK(7); POST(28); }, NOP, NOP);
      TILE(2, SA, SB, { WV(10, K2b); WV(10, Kf[3]); FIXK(); }, NOP, NOP, PRIO(1));
      TILE(3, SB, SA, WV(8, Kf[4]), NOP, NOP, NOP);
      TILE(4, SA, SB, WV(5, Kf[5]), NOP, NOP, { POST(20); PRIO(0); });
      TILE(5, SB, SA, WV(3, Kf[6]), NOP, FLAGRD(20), { CHECK(20, 12); PRIO(2); RQA(); });
      TILE(6, SA, SB, { WV(0, Kf[7]); RQB(); }, RQ1(), NOP, NOP);
      TILE(7, SB, SA, QINC(), RTAB(), NOP, { PIN(rsrcv); PIN(rdstv); });
    }
    for (int g = 1; g < 8; ++g) {
      constexpr bool FIRST = false;
      if (g == 6) asm volatile("s_waitcnt vmcnt(0)" ::: "memory");
      TILE(0, SA, SB, { rt_store(g + 6); PRIO(1); }, NOP, NOP, NOP);
      TILE(1, SB, SA, { WP0(); WP1(); }, { WP2(); POST(0); PRIO(0); }, NOP, NOP);
      TILE(2, SA, SB, FLAGRD(0), { CHECK(0, 8 * g); PRIO(2); rt_load(g); }, NOP, NOP);
      TILE(3, SB, SA, NOP, NOP, NOP, NOP);
      TILE(4, SA, SB, PRIO(1), NOP, NOP, NOP);
      TILE(5, SB, SA, RQA(), { asm volatile("s_waitcnt vmcnt(2)" ::: "memory"); POST(16); PRIO(0); }, NOP, NOP);
      TILE(6, SA, SB, { RQB(); FLAGRD(16); }, { RQ1(); CHECK(16, 12 * g); PRIO(2); }, NOP, NOP);
      TILE(7, SB, SA, QINC(), RTR(), NOP, NOP);
    }
    PRIO(0);
    Phi[3] = pk2(e14, e15);
    Oold = mfma32(VOP(0, 15), BF8(Phi), O);
    WP0(); WP1(); WP2(); POST(0);
    if (w == 0) { poll_line(ws + WS_FW + fs * 128, lane); lds_arrive(L + L2_SY + 24, lane); }
  } else {
    const int hw = w - 8, ht = tid - 512;
    if (ht < 4) *(LAS u32x4*)(L + L2_SY + ht * 16) = u32x4{0u, 0u, 0u, 0u};
    BAR_L();
    PRIO(3);
    glds16s<0>(ws + WS_QF + (size_t)(b * 8 + hw * 2) * 1024, l16, lds0 + L2_Q + hw * 2048);
    glds16s<1024>(ws + WS_QF + (size_t)(b * 8 + hw * 2) * 1024, l16, lds0 + L2_Q + hw * 2048);
    if (hw < 2) glds16s<0>(ws + WS_QF2 + (size_t)b * 2048 + hw * 1024, l16, lds0 + L2_Q2 + hw * 1024);
    const __amdgpu_buffer_rsrc_t rwo = __builtin_amdgcn_make_buffer_rsrc(ws + WS_WO + hw * 18432, 0, 663552, 0x00020000);
    if (hw == 3) {
      const int i8 = lane & 7, wa = lane >> 3, ph = i8 >= 3, b0 = m + 32 * (ph ? i8 - 3 : i8), blk = b0 < 81 ? b0 : m;
      const int kb = blk / 9, nb = blk % 9;
      const int src = ph ? (fs * 576 + kb * 64) * 1152 + nb * 128 : kb * 64 * 9216 + fs * 1152 + nb * 128;
      int dst = wf_index(ph, fs, kb * 2 + (wa >> 2), nb * 4 + (wa & 3)) * 1024;
      if (i8 == 7) dst = (int)(WS_PART - WS_WF) + b * 73728 + wa * 9216;
      *(LAS u32x2*)(L + L2_SP + lane * 8) = u32x2{(unsigned)src, (unsigned)dst};
    }
    asm volatile("s_waitcnt vmcnt(0)" ::: "memory");
    lds_arrive(L + L2_SY + 20, lane);
    lds_wait<4>(L + L2_SY + 28, 8);
    {
#pragma unroll
      for (int i = 0; i < 2; ++i) glds16s<0>((const char*)x + (size_t)b * 9216 + (hw * 2 + i) * 1024, l16, lds0 + L2_X + (hw * 2 + i) * 1024);
      if (hw == 0) glds16s<0>((const char*)x + (size_t)b * 9216 + 8192, l16, lds0 + L2_X + 8192);
      f32x4 pm[2][4];
#pragma unroll
      for (int i = 0; i < 2; ++i) {
        const int t2 = ht + 256 * i, mq = t2 < 432 ? t2 : t2 + 36 * cg;
#pragma unroll
        for (int k = 0; k < 4; ++k) pm[i][k] = *(const f32x4*)(ws + WS_MP + k * 13824 + 4608 + (mq < 576 ? mq : 0) * 16);
      }
#pragma unroll
      for (int i = 0; i < 2; ++i) {
        const int t2 = ht + 256 * i;
        const f32x4 mr = mod_round((pm[i][0] + pm[i][1]) + (pm[i][2] + pm[i][3]), (t2 >= 288 && t2 < 432) ? 1.f : 0.f);
        const u32x2 mb = {pk2(mr[0], mr[1]), pk2(mr[2], mr[3])};
        *(LAS u32x2*)(L + L2_M + t2 * 8) = mb;
      }
    }
    u32x4 wf[18];
    f32x4 oacc[9];
#pragma unroll
    for (int i = 0; i < 9; ++i) oacc[i] = f32x4{0.f, 0.f, 0.f, 0.f};
#define LWO(dst, st, i) dst = __builtin_amdgcn_raw_buffer_load_b128(rwo, l16, (st) * 73728 + (i) * 1024, 0)
#pragma unroll
    for (int i = 0; i < 18; ++i) LWO(wf[i], 0, i);
      {
        const char* rb = (const char*)w1 + (m / 9) * 64 * 9216 + fs * 1152 + (m % 9) * 128;
#pragma unroll
        for (int i = 0; i < 2; ++i) {
          const int w2_ = hw * 2 + i, rr = (w2_ & 3) * 8 + (lane >> 3);
          glds16s<0>(rb, ((w2_ >> 2) * 32 + 8 * ((rr >> 2) & 3) + 4 * (rr >> 4) + (rr & 3)) * 9216 + (lane & 7) * 16, lds0 + L2_RT + w2_ * 1024);
        }
      }
    asm volatile("s_waitcnt vmcnt(0)" ::: "memory");
    const int rp = (lane & 15) < 10 ? (lane & 15) : 9, rcl = 18 * hw + 2 * rp;
    const LAS char* rsrc = L + L2_PB + (rp >> 1) * 544 + (8 * hw + (lane >> 4)) * 16 + (rp & 1) * 8;
    int odst = (lane & 15) < 9 ? L2_OB + ob_off(0, rcl, lane >> 4) : L2_SY + 12;
    const int ostr = (lane & 15) < 9 ? (rcl < 64 ? 1024 : 128) : 0, o2nd = (lane & 15) < 9 ? 64 : 0;
    const LAS char* ord = L + L2_OB + (lane >> 4) * 128 + (lane & 7) * 16;
    unsigned long long bad = 0ull;
    u32x4 oA, oB, wd[9];
    auto reduce = [&](int g) {
      f32x2 pv[10];
      lds_wait<4>(L + L2_SY, g < 7 ? 8 * (g + 1) : 8 * g + 4);
#pragma nounroll
      for (unsigned spins = 0; spins < LSPIN_CAP; ++spins) {
        const unsigned f = *(volatile LAS unsigned*)(L + L2_SY);
        asm volatile("" ::: "memory");
#pragma unroll
        for (int i = 0; i < 10; ++i) pv[i] = *(const LAS f32x2*)(rsrc + (i >> 3) * 64 + (i & 7) * 2720);
        SBAR();
        const unsigned fs_ = __builtin_amdgcn_readfirstlane(f);
        if (fs_ >= 8u * (g + 1)) break;
      }
      f32x2 sm2[2];
      sm2[0] = ((pv[0] + pv[1]) + (pv[2] + pv[3])) + (pv[4] + pv[5]);
      PIN(sm2[0]);
      SBAR();
#pragma unroll
      for (int i = 0; i < 6; ++i) pv[i] = *(const LAS f32x2*)(rsrc + 64 + (2 + i) * 2720);
      SBAR();
      sm2[0] += pv[6] + pv[7];
      sm2[1] = (((pv[8] + pv[9]) + (pv[0] + pv[1])) + (pv[2] + pv[3])) + (pv[4] + pv[5]);
#pragma unroll
      for (int k = 0; k < 2; ++k) {
        const f32x2 sm = sm2[k];
        const float l_ = __builtin_bit_cast(float, __builtin_amdgcn_mov_dpp(__builtin_bit_cast(int, sm[0]), 0x159, 0xf, 0xf, true));
        bad |= __builtin_amdgcn_fcmpf(l_, 0x1p-96f, 12  );
        const float il = __builtin_amdgcn_rcpf(l_);
        *(LAS unsigned*)(L + odst + k * o2nd) = pk2(sm[0] * il, sm[1] * il);
      }
      odst += ostr;
    };
    auto read_o = [&](int g) {
#pragma nounroll
      for (unsigned spins = 0; spins < LSPIN_CAP; ++spins) {
        const unsigned f = *(volatile LAS unsigned*)(L + L2_SY + 4);
        asm volatile("" ::: "memory");
        oA = *(const LAS u32x4*)(ord + g * 1024); oB = *(const LAS u32x4*)(ord + g * 1024 + 512);
        SBAR();
        if (__builtin_amdgcn_readfirstlane(f) >= 4u * (g + 1)) break;
        __builtin_amdgcn_s_sleep(1);
      }
    };
    auto iter = [&](int g) {
      reduce(g);
      asm volatile("" ::: "memory");
      if (lane < 2) __hip_atomic_fetch_add((LAS unsigned*)(L + L2_SY + 4 + lane * 12), 1u, __ATOMIC_RELAXED, __HIP_MEMORY_SCOPE_WORKGROUP);
      read_o(g);
#pragma unroll
      for (int i = 0; i < 18; ++i) {
        oacc[i % 9] = mfma16(wf[i], i < 9 ? oA : oB, oacc[i % 9]);
        SBAR();
        LWO(wf[i], g + 1, i);
        SBAR();
      }
    };
#pragma nounroll
    for (int g = 0; g < 4; ++g) iter(g);
#pragma unroll
    for (int i = 0; i < 5; ++i) LWO(wd[i], 8, i);
    SBAR();
    iter(4);
    oA = *(const LAS u32x4*)(ord + 8192);
#pragma unroll
    for (int i = 0; i < 5; ++i) oacc[i] = mfma16(wd[i], oA, oacc[i]);
    SBAR();
#pragma unroll
    for (int i = 0; i < 5; ++i) LWO(wd[i], 8, i < 4 ? 5 + i : 9 + i);
    glds16s<0>(ws + WS_WO + 8 * 73728 + hw * 18432 + 15360, l16, lds0 + L2_Q + hw * 1024);
    glds16s<0>(ws + WS_WO + 8 * 73728 + hw * 18432 + 16384, l16, lds0 + L2_OB + hw * 1024);
    {
      unsigned long long pc;
      int len;
      asm volatile("s_getpc_b64 %0\nadaln_code_mid:\n\ts_mov_b32 %1, adaln_code_end-adaln_code_mid" : "=s"(pc), "=s"(len));
#pragma unroll
      for (int i = 0; i < 2; ++i) {
        const int off = (ht + 256 * i) * 128;
        glds4((const char*)pc + (off < len ? off : 0), lds0 + L2_SP);
      }
    }
    SBAR();
    iter(5);
    if (tid == 512) __hip_atomic_store((GAS unsigned*)(ws + WS_FW + fs * 128 + m * 4), 1u, RLX_AGENT);
    oA = *(const LAS u32x4*)(ord + 8192);
#pragma unroll
    for (int i = 0; i < 4; ++i) oacc[5 + i] = mfma16(wd[i], oA, oacc[5 + i]);
    SBAR();
#pragma unroll
    for (int i = 0; i < 4; ++i) LWO(wd[i], 8, 9 + i);
    SBAR();
    iter(6);
    const int sp5 = hw < 2 ? L2_OB + 4096 + hw * 1024 : L2_SP + (hw - 2) * 1024;
    glds16s<0>(ws + WS_WO + 8 * 73728 + hw * 18432 + 14336, l16, lds0 + sp5);
    glds16s<0>(ws + WS_WO + 8 * 73728 + hw * 18432 + 17408, l16, lds0 + L2_Q + 4096 + hw * 1024);
    SBAR();
    reduce(7);
    if (bad != 0ull) *(LAS unsigned*)(L + L2_SY + 8) = 1u;
    lds_arrive(L + L2_SY + 4, lane);
    asm volatile("s_waitcnt vmcnt(0)" ::: "memory");
    read_o(7);
    const unsigned redo = *(volatile LAS unsigned*)(L + L2_SY + 8);
    {
#pragma unroll
      for (int i = 0; i < 9; ++i) { oacc[i] = mfma16(wf[i], oA, oacc[i]); SBAR(); }
      const u32x4 oD = *(const LAS u32x4*)(ord + 8704);
    wd[5] = *(const LAS u32x4*)(L + sp5 + l16);
    wd[6] = *(const LAS u32x4*)(L + L2_Q + hw * 1024 + l16); wd[7] = *(const LAS u32x4*)(L + L2_OB + hw * 1024 + l16);
    wd[8] = *(const LAS u32x4*)(L + L2_Q + 4096 + hw * 1024 + l16);
      SBAR();
#pragma unroll
      for (int i = 0; i < 9; ++i) { oacc[i] = mfma16(wf[9 + i], oB, oacc[i]); SBAR(); }
#pragma unroll
      for (int i = 0; i < 9; ++i) { oacc[i] = mfma16(wd[i], oD, oacc[i]); PIN(oacc[i]); }
    }
    if (__builtin_amdgcn_readfirstlane(redo) != 0u) {
      BAR_L();
      BAR_L();
#pragma unroll
      for (int i = 0; i < 9; ++i) oacc[i] = f32x4{0.f, 0.f, 0.f, 0.f};
#pragma nounroll
      for (int s2 = 0; s2 < 18; ++s2) {
        const u32x4 of = *(const LAS u32x4*)(ord + s2 * 512);
#pragma unroll
        for (int i = 0; i < 9; ++i)
          oacc[i] = mfma16(__builtin_amdgcn_raw_buffer_load_b128(rwo, l16, (s2 >> 1) * 73728 + ((s2 & 1) * 9 + i) * 1024, 0), of, oacc[i]);
      }
    }
    if ((lane & 8) == 0) {
      LAS char* eb = L + L2_PB + (lane & 7) * 2320 + (lane >> 4) * 16 + hw * 576;
#pragma unroll
      for (int i = 0; i < 9; ++i) *(LAS f32x4*)(eb + i * 64) = oacc[i];
    }
    PRIO(0);
  }
  constexpr int RING = 18;
  u32x4 wg[RING];
  const __amdgpu_buffer_rsrc_t rws = __builtin_amdgcn_make_buffer_rsrc(ws + WS_WF + (size_t)(fs * 12 + w) * 110592, 0, 110592, 0x00020000);
#define LW(i) { if ((i) < 108) wg[(i) % RING] = __builtin_amdgcn_raw_buffer_load_b128(rws, l16, (i) * 1024, AUX_SC1); }
  if (w >= 8) BAR_L();
  lds_wait(L + L2_SY + 24, 1);
#pragma unroll
  for (int i = 0; i < PRE1; ++i) LW(i);
  SBAR();
  if (w < 8) {
  const u32x4 xv = *(const LAS u32x4*)(L + L2_X + w * 1152 + l16), gv = *(const LAS u32x4*)(L + L2_M + l16);
  const unsigned x8 = *(const LAS u16*)(L + L2_X + w * 1152 + 1024 + lane * 2), g8 = *(const LAS u16*)(L + L2_M + 1024 + lane * 2);
  const u32x4 sv = *(const LAS u32x4*)(L + L2_M + 1152 + l16), cv = *(const LAS u32x4*)(L + L2_M + 2304 + l16);
  const unsigned s8 = *(const LAS u16*)(L + L2_M + 1152 + 1024 + lane * 2), c8 = *(const LAS u16*)(L + L2_M + 2304 + 1024 + lane * 2);
  f32x2 xf[4], gf[4], sf[4], cf[4];
#pragma unroll
  for (int j = 0; j < 4; ++j) {
    xf[j] = f32x2{bflo(xv[j]), bfhi(xv[j])}; gf[j] = f32x2{bflo(gv[j]), bfhi(gv[j])};
    sf[j] = f32x2{bflo(sv[j]), bfhi(sv[j])}; cf[j] = f32x2{bflo(cv[j]), bfhi(cv[j])};
    PIN(xf[j]); PIN(gf[j]); PIN(sf[j]); PIN(cf[j]);
  }
  BAR_L();
  if (__builtin_amdgcn_readfirstlane(*(volatile LAS unsigned*)(L + L2_SY + 8)) != 0u) {
    const int rw = tid >> 1, hf = tid & 1;
    const int gb = rw >> 5, l32 = rw & 31;
    float qv[18];
    {
      const u16* qa = (const u16*)(ws + WS_QF + (size_t)(b * 8 + gb) * 1024);
      for (int j = 0; j < 8; ++j) {
        qv[(j & 3) + 8 * (j >> 2)] = bf2f(qa[l32 * 8 + j]);
        qv[(j & 3) + 8 * (j >> 2) + 4] = bf2f(qa[(32 + l32) * 8 + j]);
      }
      const unsigned q2v = *(const LAS unsigned*)(L + L2_Q2 + gb * 256 + l32 * 4);
      qv[16] = bflo(q2v); qv[17] = bfhi(q2v);
    }
    float mx = -INFINITY, l = 0.f, ov[18];
#pragma unroll
    for (int d = 0; d < 18; ++d) ov[d] = 0.f;
    for (int pass = 0; pass < 2; ++pass) {
      for (int key = hf * 1024; key < hf * 1024 + 1024; ++key) {
        const int T = key >> 5, kr = key & 31;
        const u32x4 ka_ = *(const u32x4*)(ws + WS_KF + ((size_t)T * 64 + kr) * 16);
        const u32x4 kb_ = *(const u32x4*)(ws + WS_KF + ((size_t)T * 64 + 32 + kr) * 16);
        const unsigned kc_ = *(const unsigned*)(ws + WS_KF2 + (((T >> 3) * 32 + kr) * 8 + (T & 7)) * 4);
        float s = qv[16] * bflo(kc_) + qv[17] * bfhi(kc_);
#pragma unroll
        for (int j = 0; j < 4; ++j) {
          const int d0 = (2 * j & 3) + 8 * (j >> 1);
          s += qv[d0] * bflo(ka_[j]) + qv[d0 + 1] * bfhi(ka_[j]) + qv[d0 + 4] * bflo(kb_[j]) + qv[d0 + 5] * bfhi(kb_[j]);
        }
        if (pass == 0) mx = fmaxf(mx, s);
        else {
          const float p = rbf(EX2(s - mx));
          l += p;
          const int st = kr >> 4, hh = (kr >> 2) & 1, j = ((kr >> 3) & 1) * 4 + (kr & 3);
          const u16* vp = (const u16*)(ws + WS_VF + (T >> 3) * 11264 + (((T & 7) * 2 + st) * 2 + hh) * 336) + j;
#pragma unroll
          for (int d = 0; d < 18; ++d) ov[d] += p * bf2f(vp[d * 8]);
        }
      }
      if (pass == 0) mx = fmaxf(mx, __shfl_xor(mx, 1));
    }
    l += __shfl_xor(l, 1);
    const float il = 1.f / l;
#pragma unroll
    for (int d = 0; d < 18; ++d) ov[d] = (ov[d] + __shfl_xor(ov[d], 1)) * il;
    if (hf == 0) {
#pragma unroll
      for (int d = 0; d < 18; d += 2) *(LAS unsigned*)(L + L2_OB + ob_off(gb, 18 * (l32 >> 3) + d, l32 & 7)) = pk2(ov[d], ov[d + 1]);
    }
    BAR_L();
    BAR_L();
#pragma unroll
    for (int i = 0; i < PRE1; ++i) LW(i);
  }
  {
    const LAS char* er = L + L2_PB + w * 2320;
    const f32x4 a0 = *(const LAS f32x4*)(er + lane * 32), a1 = *(const LAS f32x4*)(er + lane * 32 + 16);
    const float a8 = *(const LAS float*)(er + 2048 + lane * 4);
    u32x4 xp;
    xp[0] = pk2(fmaf(gf[0][0], a0[0], xf[0][0]), fmaf(gf[0][1], a0[1], xf[0][1]));
    xp[1] = pk2(fmaf(gf[1][0], a0[2], xf[1][0]), fmaf(gf[1][1], a0[3], xf[1][1]));
    xp[2] = pk2(fmaf(gf[2][0], a1[0], xf[2][0]), fmaf(gf[2][1], a1[1], xf[2][1]));
    xp[3] = pk2(fmaf(gf[3][0], a1[2], xf[3][0]), fmaf(gf[3][1], a1[3], xf[3][1]));
    const unsigned xp8 = pk2(fmaf(bflo(g8), a8, bflo(x8)), 0.f);
    *(LAS u32x4*)(L + L2_X + w * 1152 + l16) = xp;
    *(LAS u16*)(L + L2_X + w * 1152 + 1024 + lane * 2) = (u16)xp8;
    f32x2 v[4];
#pragma unroll
    for (int j = 0; j < 4; ++j) v[j] = f32x2{bflo(xp[j]), bfhi(xp[j])};
    float v8 = bflo(xp8);
    const f32x2 s2 = (v[0] + v[1]) + (v[2] + v[3]);
    const float mean = wave_sum(s2[0] + s2[1] + v8) * (1.f / D_);
    const float mb_ = rbf(mean);
    v8 -= mean;
    f32x2 m2 = {v8 * v8, 0.f};
#pragma unroll
    for (int j = 0; j < 4; ++j) { const f32x2 d_ = v[j] - mean; m2 = __builtin_elementwise_fma(d_, d_, m2); v[j] = (v[j] - mb_) * cf[j]; }
    v8 += mean - mb_;
    const float rstd = ln_rstd(wave_sum(m2[0] + m2[1]) * (1.f / D_));
    u32x4 hp;
#pragma unroll
    for (int j = 0; j < 4; ++j) hp[j] = pk2(fmaf(v[j][0], rstd, sf[j][0]), fmaf(v[j][1], rstd, sf[j][1]));
    const unsigned hp8 = pk2(fmaf(v8 * rstd, bflo(c8), bflo(s8)), 0.f);
    *(LAS u32x4*)(L + L2_OB + lane * 128 + ((w ^ lane) & 7) * 16) = hp;
    *(LAS u16*)(L + L2_OB + (64 + (lane >> 3)) * 128 + ((w ^ (lane >> 3)) & 7) * 16 + (lane & 7) * 2) = (u16)hp8;
  }
  }
  BAR_L();
  const __amdgpu_buffer_rsrc_t rh = __builtin_amdgcn_make_buffer_rsrc(ws + WS_H2 + (size_t)m * 73728, 0, 73728, 0x00020000);
  if (w < 9) {
    st128(*(const LAS u32x4*)(L + L2_OB + (tid >> 3) * 128 + ((tid ^ (tid >> 3)) & 7) * 16), rh,
                                           (tid >> 3) * 256 + (tid & 7) * 16, cg * 18432 + jo * 128, AUX_SC1);
    asm volatile("s_waitcnt vmcnt(0)" ::: "memory");
  }
  asm volatile("s_barrier" ::: "memory");
  if (w == 0) {
    if (lane < 9)
      __hip_atomic_store((GAS unsigned*)(ws + (lane < 8 ? WS_CNT + m * 4096 + lane * 512 + 256 + fs * 16 : WS_FW + 1024 + fs * 128 + m * 4)), 1u,
                         RLX_AGENT);
    poll_flags(ws + WS_CNT + m * 4096 + fs * 512 + 256, lane);
  }
  if (w >= 3) st128(*(const LAS u32x4*)(L + L2_X + (tid - 192) * 16),
                                           __builtin_amdgcn_make_buffer_rsrc(ws + WS_X1 + (size_t)b * 9216, 0, 9216, 0x00020000), (tid - 192) * 16, 0, AUX_SC1);
  LAS char* stash = L + L3_H + w * 6144 + l16;
#pragma unroll
  for (int i = 0; i < 6; ++i) *(LAS u32x4*)(stash + i * 1024) = wg[i];
  SBAR();
#pragma unroll
  for (int i = PRE1; i < 18; ++i) LW(i);
  SBAR();
  asm volatile("s_barrier" ::: "memory");
  u32x4 ag[6];
  const int ao = ((w & 3) * 18 + (w >> 2)) * 1024;
#define GA(i) ag[i] = __builtin_amdgcn_raw_buffer_load_b128(rh, l16, ao + (i) * 3072, AUX_SC1)
#define WA(i) *(LAS u32x4*)(L + L3_A + ao + (i) * 3072 + l16) = ag[i]
  GA(0);
  SBAR();
  asm volatile("s_barrier" ::: "memory");
  SBAR();
  GA(1); GA(2);
  SBAR();
  WA(0);
  SBAR();
  GA(3); GA(4); GA(5);
  SBAR();
  BAR_L();
  SBAR();
  f32x4 acc[3][4];
  u32x4 af[2][4];
  const LAS char* ab = L + L3_A + l16;
#define RA(buf, s) { _Pragma("unroll") for (int j = 0; j < 4; ++j) af[buf][j] = *(const LAS u32x4*)(ab + (j * 18 + (s)) * 1024); }
#define ZACC(i) { _Pragma("unroll") for (int j = 0; j < 4; ++j) acc[i][j] = f32x4{0.f, 0.f, 0.f, 0.f}; }
#define MM(i, fi, buf) { const u32x4 c_ = wg[(fi) % RING]; _Pragma("unroll") for (int j = 0; j < 4; ++j) acc[i][j] = mfma16(c_, af[buf][j], acc[i][j]); }
  ZACC(0); ZACC(1); ZACC(2);
#pragma unroll
  for (int i = 0; i < 3; ++i) wg[i] = *(const LAS u32x4*)(stash + i * 1024);
  RA(0, 0);
  SBAR();
#pragma unroll
  for (int s = 0; s < 18; ++s) {
    if (s == 0) PRIO(3); if (s == 5) PRIO(2); if (s == 10) PRIO(1); if (s == 14) PRIO(0);
    if (s == 7 && w == 0) poll_line(ws + WS_FW + 1024 + fs * 128, lane);
    if (s == 2) { WA(1); WA(2); BAR_L(); }
    if (s == 5) { WA(3); WA(4); WA(5); }
    if (s == 8) BAR_L();
    if (s < 17) RA((s + 1) & 1, s + 1);
    SBAR();
#pragma unroll
    for (int i = 0; i < 3; ++i) {
      MM(i, s * 3 + i, s & 1);
      SBAR();
      { const int n = s * 3 + i;
        if (n < 3) wg[n + 3] = *(const LAS u32x4*)(stash + (n + 3) * 1024);
        else if (n < 6) {}
        else if (n < 24) LW(n + 12) else if (n < 27) { LW(36 + 2 * (n - 24)); LW(37 + 2 * (n - 24)); } else LW(n + 15); }
      SBAR();
    }
  }
#pragma unroll
  for (int i = 0; i < 3; ++i) {
    const int t = w * 3 + i;
#pragma unroll
    for (int j = 0; j < 4; ++j) {
      const u32x2 o = {relu2(pk2(acc[i][j][0], acc[i][j][1])), relu2(pk2(acc[i][j][2], acc[i][j][3]))};
      *(LAS u32x2*)(L + L3_H + (j * 18 + (t >> 1)) * 1024 + l16 + (t & 1) * 8) = o;
    }
  }
  BAR_L();
  SBAR();
  ab = L + L3_H + l16;
  ZACC(0); ZACC(1);
  RA(0, 0);
  SBAR();
#pragma unroll
  for (int s = 0; s < 18; ++s) {
    if (s == 0) PRIO(3); if (s == 6) PRIO(2); if (s == 12) PRIO(1);
    RA((s + 1) & 1, (s + 1) % 18);
    SBAR();
#pragma unroll
    for (int i = 0; i < 2; ++i) {
      MM(i, 54 + s * 2 + i, s & 1);
      SBAR();
      if (s * 2 + i < 3) { LW(69 + 2 * (s * 2 + i)); LW(70 + 2 * (s * 2 + i)); } else LW(54 + s * 2 + i + RING);
      SBAR();
    }
  }
  const __amdgpu_buffer_rsrc_t rs = __builtin_amdgcn_make_buffer_rsrc(ws + WS_PART + (size_t)m * 589824, 0, 589824, 0x00020000);
#define PUB(i, jp, t)                                                                                                        \
  st128(                                                                                    \
      u32x4{pk2(acc[i][2 * (jp)][0], acc[i][2 * (jp)][1]), pk2(acc[i][2 * (jp)][2], acc[i][2 * (jp)][3]),                    \
            pk2(acc[i][2 * (jp) + 1][0], acc[i][2 * (jp) + 1][1]), pk2(acc[i][2 * (jp) + 1][2], acc[i][2 * (jp) + 1][3])},   \
      rs, l16, ((fs * 2 + (jp)) * 36 + (t)) * 1024, AUX_SC1)
  const int prow = tid / 18, pc = tid % 18;
  const int ooff = (jo * 32 + (tid < 576 ? prow : 0)) * 1152 + cg * 288 + pc * 16;
  u32x4 xv;
  SBAR();
  asm volatile("s_barrier" ::: "memory");
  SBAR();
  ZACC(2);
  const int t0 = (9 * cg + 12 + 2 * w) % 36, t1 = (9 * cg + 13 + 2 * w) % 36;
#pragma unroll
  for (int s = 0; s < 18; ++s) {
    if (s == 0) PRIO(3); if (s == 5) PRIO(2); if (s == 10) PRIO(1); if (s == 14) PRIO(0);
    if (s < 17) RA((s + 1) & 1, s + 1);
    SBAR();
    if (s == 0) { PUB(0, 0, t0); PUB(0, 1, t0); }
    if (s == 1) { PUB(1, 0, t1); PUB(1, 1, t1); }
    if (s == K3_SIG) {
      asm volatile("s_waitcnt vmcnt(0)" ::: "memory");
      wg_signal(L + L3_FLG, ws + WS_CNT + m * 4096 + fs * 16, lane, 11u);
    }
    SBAR();
    MM(2, 90 + s, s & 1);
    SBAR();
  }
#define ACC8(v) { sa += f32x4{bflo(v[0]), bfhi(v[0]), bflo(v[1]), bfhi(v[1])}; sb += f32x4{bflo(v[2]), bfhi(v[2]), bflo(v[3]), bfhi(v[3])}; }
  {
    const int t2 = (9 * cg + w) % 36;
    if (w >= 9 || jo == 1) PUB(2, 0, t2);
    if (w >= 9 || jo == 0) PUB(2, 1, t2);
  }
  if (w == 0) { poll_flags(ws + WS_CNT + m * 4096 + fs * 512, lane); lds_arrive(L + L3_FLG + 12, lane); }
  asm volatile("s_waitcnt vmcnt(0)" ::: "memory");
  wg_signal(L + L3_FLG + 4, ws + WS_CNT + m * 4096 + 128 + fs * 16, lane, 11u);
  if (w < 9) {
    const int ub = (jo * 36 + 9 * cg + w) * 1024, pb = (fs & ~1);
    const int big = 1 << 30;
    u32x4 pv[9];
    lds_wait(L + L3_FLG + 12, 1);

#pragma unroll
    for (int i = 0; i < 6; ++i)
      pv[i] = __builtin_amdgcn_raw_buffer_load_b128(rs, (i >= 4 && w < 3) ? big : l16, ((pb + 2 + i) & 7) * 73728 + ub, AUX_SC1);
    xv = __builtin_amdgcn_raw_buffer_load_b128(__builtin_amdgcn_make_buffer_rsrc(ws + WS_X1 + (size_t)m * 73728, 0, 73728, 0x00020000), ooff, 0,
                                               AUX_SC1);
    SBAR();
    f32x4 sa = jo ? acc[2][2] : acc[2][0], sb = jo ? acc[2][3] : acc[2][1];
#pragma unroll
    for (int f = 0; f < 4; ++f) ACC8(pv[f]);
    PIN(sa); PIN(sb);
    SBAR();
    lds_wait(L + L3_FLG + 8, 1);
    pv[6] = __builtin_amdgcn_raw_buffer_load_b128(rs, l16, (fs ^ 1) * 73728 + ub, AUX_SC1);
    if (w < 3) {
#pragma unroll
      for (int i = 4; i < 6; ++i) pv[i] = __builtin_amdgcn_raw_buffer_load_b128(rs, l16, ((pb + 2 + i) & 7) * 73728 + ub, AUX_SC1);
    }
#pragma unroll
    for (int f = 4; f < 7; ++f) ACC8(pv[f]);
    LAS char* tb = L + (lane & 15) * 592 + w * 64 + (lane >> 4) * 16;
    *(LAS f32x4*)tb = sa;
    *(LAS f32x4*)(tb + 16 * 592) = sb;
  } else if (w == 9) {
    poll_flags(ws + WS_CNT + m * 4096 + fs * 512 + 128, lane);
    lds_arrive(L + L3_FLG + 8, lane);
  }
  BAR_L();
  if (w < 9) {
    const u32x4 gv = *(const LAS u32x4*)(L + L2_M + 3456 + pc * 16);
    const f32x4 y0 = *(const LAS f32x4*)(L + prow * 592 + pc * 32), y1 = *(const LAS f32x4*)(L + prow * 592 + pc * 32 + 16);
    const u32x4 o = {pk2(fmaf(bflo(gv[0]), y0[0], bflo(xv[0])), fmaf(bfhi(gv[0]), y0[1], bfhi(xv[0]))),
                     pk2(fmaf(bflo(gv[1]), y0[2], bflo(xv[1])), fmaf(bfhi(gv[1]), y0[3], bfhi(xv[1]))),
                     pk2(fmaf(bflo(gv[2]), y1[0], bflo(xv[2])), fmaf(bfhi(gv[2]), y1[1], bfhi(xv[2]))),
                     pk2(fmaf(bflo(gv[3]), y1[2], bflo(xv[3])), fmaf(bfhi(gv[3]), y1[3], bfhi(xv[3])))};
    st128(o, __builtin_amdgcn_make_buffer_rsrc((char*)out + (size_t)m * 73728, 0, 73728, 0x00020000), ooff, 0, AUX_SC1);
  }
  asm volatile("adaln_code_end:");
}

extern "C" void kernel_launch(void* const* d_in, const int* in_sizes, int n_in, void* d_out, int out_size, void* d_ws,
                              size_t ws_size, hipStream_t stream) {
  const u16* x = (const u16*)d_in[0];
  const u16* wq = (const u16*)d_in[1];
  const u16* wk = (const u16*)d_in[2];
  const u16* wv = (const u16*)d_in[3];
  const u16* wo = (const u16*)d_in[4];
  const u16* w1 = (const u16*)d_in[6];
  const u16* w2 = (const u16*)d_in[7];
  const u16* c = (const u16*)d_in[8];
  const u16* wmod = (const u16*)d_in[9];
  char* ws = (char*)d_ws;
  (void)hipFuncSetAttribute((const void*)k2_attn, hipFuncAttributeMaxDynamicSharedMemorySize, L2_BYTES);
  (void)hipFuncSetAttribute((const void*)k1_qkv, hipFuncAttributeMaxDynamicSharedMemorySize, L1_BYTES);
  k1_qkv<<<256, 768, L1_BYTES, stream>>>(x, wq, wk, wv, wo, c, wmod, ws);
  k2_attn<<<256, 768, L2_BYTES, stream>>>(x, w1, w2, ws, (u16*)d_out);
}
```
